```python
import torch

def _softmax(x, axis=-1):
    return torch.softmax(x, dim=axis)
def _gelu(x):
    return torch.nn.functional.gelu(x, approximate="tanh")
def _silu(x):
    return torch.nn.functional.silu(x)
def _sigmoid(x):
    return torch.sigmoid(x)
def _layernorm(x, axis=-1, eps=1e-5):
    m = x.mean(dim=axis, keepdim=True)
    v = x.var(dim=axis, keepdim=True, unbiased=False)
    return (x - m) / torch.sqrt(v + eps)
def _scatter_add(tgt, idx, src, axis=0):
    return tgt.clone().index_add_(axis, idx, src)
def _segment_sum(src, seg, n):
    out = torch.zeros((n,) + src.shape[1:], dtype=src.dtype)
    return out.index_add_(0, seg, src)
def _pad(x, pw):
    flat = tuple(v for pair in reversed(pw) for v in pair)
    return torch.nn.functional.pad(x, flat)

kernel_name = 'sequential_preLN__linear_kernel_hedgehog__dense_activation_mish__prompt'

def setup_inputs():

    _w = (lambda a: (lambda t: t.to(torch.bfloat16) if t.is_floating_point() else t)(torch.as_tensor(a)))
    def _A_setup():
        B, S, D, H = 1, 2048, 448, 16
        Dh = D // H
        return {
            "x": _w((lambda *s: torch.randn(*s, dtype=torch.bfloat16) * 0.05)(B, S, D)),
            "wqkv": _w((lambda *s: torch.randn(*s, dtype=torch.bfloat16) * 0.2)(D, 3 * D)),
            "w_phi": _w((lambda *s: torch.randn(*s, dtype=torch.bfloat16) * 0.2)(H, Dh, Dh) * (Dh ** -0.5)),
            "wo": _w((lambda *s: torch.randn(*s, dtype=torch.bfloat16) * 0.05)(D, D)),
        }
    _A_in = _A_setup()

    def _B_setup():
        B, S, D, F = 1, 2048, 448, 1792
        return {
            "x": _w((lambda *s: torch.randn(*s, dtype=torch.bfloat16) * 0.05)(B, S, D)),
            "w1": _w((lambda *s: torch.randn(*s, dtype=torch.bfloat16) * 1.6)(D, F) * (D ** -0.5)),
            "b1": _w((lambda *s: torch.randn(*s, dtype=torch.bfloat16) * 0.4)(F)),
            "w2": _w((lambda *s: torch.randn(*s, dtype=torch.bfloat16) * 0.1)(F, D) * (F ** -0.5)),
        }
    _B_in = _B_setup()

    return {
        "A_x": _A_in["x"],
        "A_wqkv": _A_in["wqkv"],
        "A_w_phi": _A_in["w_phi"],
        "A_wo": _A_in["wo"],
        "B_x": _B_in["x"],
        "B_w1": _B_in["w1"],
        "B_b1": _B_in["b1"],
        "B_w2": _B_in["w2"],
    }

def reference(A_x, A_wqkv, A_w_phi, A_wo, B_x, B_w1, B_b1, B_w2):
    def _A_ref(x, wqkv, w_phi, wo):
        B, S, D = x.shape
        H = 16; Dh = D // H
        qkv = (lambda x, *d: x.permute(*d))((x @ wqkv).reshape(B, S, 3, H, Dh), 2, 0, 3, 1, 4)
        q, k, v = qkv[0], qkv[1], qkv[2]
        pq = torch.einsum("bhsd,hdf->bhsf", q, w_phi)
        pk = torch.einsum("bhsd,hdf->bhsf", k, w_phi)
        phi_q = torch.concatenate([_softmax(pq, axis=-1), _softmax(-pq, axis=-1)], axis=-1)
        phi_k = torch.concatenate([_softmax(pk, axis=-1), _softmax(-pk, axis=-1)], axis=-1)
        kv = torch.einsum("bhsf,bhsd->bhfd", phi_k, v)
        z  = torch.einsum("bhsf->bhf", phi_k)
        num = torch.einsum("bhqf,bhfd->bhqd", phi_q, kv)
        den = torch.einsum("bhqf,bhf->bhq", phi_q, z)[..., None] + 1e-6
        o = num / den
        return (lambda x, *d: x.permute(*d))(o, 0, 2, 1, 3).reshape(B, S, D) @ wo

    def _B_ref(x, w1, b1, w2):
        h = torch.einsum("bsd,df->bsf", x, w1) + b1
        sp = torch.log1p(torch.exp(-torch.abs(h))) + torch.where(h > 0, h, torch.zeros_like(h))
        act = h * torch.tanh(sp)
        return torch.einsum("bsf,fd->bsd", act, w2)

    x = A_x
    h = x + _A_ref(_layernorm(x, axis=-1), A_wqkv, A_w_phi, A_wo)
    h2 = h + _B_ref(_layernorm(h, axis=-1), B_w1, B_b1, B_w2)
    return h2
```

```cpp
#include <hip/hip_runtime.h>
#include <hip/hip_bf16.h>
typedef unsigned short u16;
__device__ __forceinline__ float b2f(u16 v){ return __uint_as_float(((unsigned)v)<<16); }
__device__ __forceinline__ u16 f2b(float f){ __hip_bfloat16 h = __float2bfloat16(f); return *(u16*)&h; }
constexpr int S=2048,D=448,H=16,DH=28,F=1792;

template<bool F32IN>
__global__ void ln_k(const void* xin, float* y){
  int r=blockIdx.x; int l=threadIdx.x; float v[7]; float s=0;
  for(int i=0;i<7;i++){ v[i]= F32IN? ((const float*)xin)[r*D+l+64*i] : b2f(((const u16*)xin)[r*D+l+64*i]); s+=v[i]; }
  for(int o=32;o;o>>=1) s+=__shfl_xor(s,o);
  float m=s/D; float q=0; for(int i=0;i<7;i++){ q+=(v[i]-m)*(v[i]-m);}
  for(int o=32;o;o>>=1) q+=__shfl_xor(q,o);
  float rs=rsqrtf(q/D+1e-5f);
  for(int i=0;i<7;i++) y[r*D+l+64*i]=(v[i]-m)*rs;
}
__global__ void gemm_k(const float* A,const u16* B,float* C,u16* Cb,const u16* bias,const u16* resb,const float* resf,int N,int K,int mode){
  __shared__ float As[32][33]; __shared__ float Bs[32][33];
  int tx=threadIdx.x&31, ty=threadIdx.x>>5; int n0=blockIdx.x*32, m0=blockIdx.y*32;
  float acc[4]={0,0,0,0};
  for(int k0=0;k0<K;k0+=32){
    for(int i=0;i<4;i++){ As[ty+8*i][tx]=A[(m0+ty+8*i)*K+k0+tx]; Bs[ty+8*i][tx]=b2f(B[(k0+ty+8*i)*N+n0+tx]); }
    __syncthreads();
    for(int k=0;k<32;k++){ float b=Bs[k][tx]; for(int i=0;i<4;i++) acc[i]+=As[ty+8*i][k]*b; }
    __syncthreads();
  }
  for(int i=0;i<4;i++){ int m=m0+ty+8*i,n=n0+tx; float v=acc[i];
    if(mode==1){ v+=b2f(bias[n]); float sp=log1pf(expf(-fabsf(v)))+fmaxf(v,0.f); v=v*tanhf(sp); C[m*N+n]=v; }
    else if(mode==2){ C[m*N+n]=v+b2f(resb[m*N+n]); }
    else if(mode==3){ Cb[m*N+n]=f2b(v+resf[m*N+n]); }
    else C[m*N+n]=v; }
}
__global__ void phi_k(const float* qkv,const u16* wphi,float* phi){
  int s=blockIdx.x; int wh=blockIdx.y; int which=wh/H,h=wh%H; int l=threadIdx.x;
  float p=0; if(l<DH){ for(int d=0;d<DH;d++) p+=qkv[s*3*D+which*D+h*DH+d]*b2f(wphi[(h*DH+d)*DH+l]); }
  float mx= l<DH? p:-1e30f, mn= l<DH? -p:-1e30f;
  for(int o=32;o;o>>=1){ mx=fmaxf(mx,__shfl_xor(mx,o)); mn=fmaxf(mn,__shfl_xor(mn,o)); }
  float e1= l<DH? expf(p-mx):0, e2= l<DH? expf(-p-mn):0; float s1=e1,s2=e2;
  for(int o=32;o;o>>=1){ s1+=__shfl_xor(s1,o); s2+=__shfl_xor(s2,o);}
  if(l<DH){ float* o=phi+((size_t)(which*H+h)*S+s)*56; o[l]=e1/s1; o[28+l]=e2/s2; }
}
__global__ void kv_k(const float* qkv,const float* phi,float* kv){
  int h=blockIdx.x/56,f=blockIdx.x%56; int l=threadIdx.x; int d=l&31, half=l>>5; float a=0;
  for(int s=half;s<S;s+=2){ float p=phi[((size_t)(H+h)*S+s)*56+f]; float v= d<DH? qkv[s*3*D+2*D+h*DH+d] : 1.f; a+=p*v; }
  a+=__shfl_xor(a,32); if(l<32) kv[(h*56+f)*32+d]=a;
}
__global__ void o_k(const float* phi,const float* kv,float* o){
  int s=blockIdx.x,h=blockIdx.y; int l=threadIdx.x; int d=l&31; float a=0;
  for(int f=0;f<56;f++) a+=phi[((size_t)h*S+s)*56+f]*kv[(h*56+f)*32+d];
  float den=__shfl(a,28)+1e-6f; if(l<DH) o[s*D+h*DH+l]=a/den;
}
extern "C" void kernel_launch(void* const* d_in, const int* in_sizes, int n_in, void* d_out, int out_size, void* d_ws, size_t ws_size, hipStream_t stream) {
  const u16* x=(const u16*)d_in[0]; const u16* wqkv=(const u16*)d_in[1]; const u16* wphi=(const u16*)d_in[2]; const u16* wo=(const u16*)d_in[3];
  const u16* w1=(const u16*)d_in[5]; const u16* b1=(const u16*)d_in[6]; const u16* w2=(const u16*)d_in[7];
  float* ws=(float*)d_ws; float* ln=ws; float* qkv=ln+S*D; float* phi=qkv+S*3*D; float* kv=phi+2*H*S*56; float* o=kv+H*56*32; float* h=o+S*D; float* act=h+S*D;
  ln_k<false><<<S,64,0,stream>>>(x,ln);
  gemm_k<<<dim3(3*D/32,S/32),256,0,stream>>>(ln,wqkv,qkv,nullptr,nullptr,nullptr,nullptr,3*D,D,0);
  phi_k<<<dim3(S,2*H),64,0,stream>>>(qkv,wphi,phi);
  kv_k<<<H*56,64,0,stream>>>(qkv,phi,kv);
  o_k<<<dim3(S,H),64,0,stream>>>(phi,kv,o);
  gemm_k<<<dim3(D/32,S/32),256,0,stream>>>(o,wo,h,nullptr,nullptr,x,nullptr,D,D,2);
  ln_k<true><<<S,64,0,stream>>>(h,ln);
  gemm_k<<<dim3(F/32,S/32),256,0,stream>>>(ln,w1,act,nullptr,b1,nullptr,nullptr,F,D,1);
  gemm_k<<<dim3(D/32,S/32),256,0,stream>>>(act,w2,nullptr,(u16*)d_out,nullptr,nullptr,h,D,F,3);
}
```

```cpp
#pragma clang diagnostic ignored "-Wint-to-pointer-cast"
#include <hip/hip_runtime.h>
#include <hip/hip_bf16.h>

typedef unsigned u32;
typedef unsigned long long u64;
typedef u32 u32x2 __attribute__((ext_vector_type(2)));
typedef u32 u32x4 __attribute__((ext_vector_type(4)));
typedef float f32x2 __attribute__((ext_vector_type(2)));
typedef float f32x4 __attribute__((ext_vector_type(4)));
typedef float f32x16 __attribute__((ext_vector_type(16)));
typedef __bf16 bf16x2 __attribute__((ext_vector_type(2)));
typedef __bf16 bf16x4 __attribute__((ext_vector_type(4)));
typedef __bf16 bf16x8 __attribute__((ext_vector_type(8)));
#define LAS __attribute__((address_space(3)))
#define GAS __attribute__((address_space(1)))
#define DI __device__ __forceinline__

constexpr u32 WS_EPOCH = 0;
constexpr u32 WS_F1 = 0x1000, WS_F2 = 0x2000, WS_F3 = 0x3000, WS_F4 = 0x5000;
constexpr u32 WS_KVP = 0x10000, WS_OB = 0x110000, WS_HB = 0x310000, WS_ACT = 0x550000, WS_END = 0xC50000;
constexpr u32 HB_STRIDE = 73728, ACT_STRIDE = 229376;
constexpr u32 L_WK = 0, L_WV = 28672, L_WQ = 57344, L_XR = 86016, L_WPHI = 151552, L_CS = 153600, L_KVS = 154112;
constexpr u32 L_RED = 86016, L_HT = 86016, L_HST = L_HT + 57344, L_RED2 = 86016, L_HRES = L_RED2 + 65536;
constexpr u32 LDS_BYTES = 160768;
constexpr u32 PV_STRIDE = 200;
constexpr float LOG2E = 1.4426950408889634f;
constexpr u32 SPIN_CAP = 200000;

__shared__ __attribute__((aligned(16))) char g_lds[LDS_BYTES];

DI u32x4 make_srd(const void* p, u32 bytes) {
  const u64 a = (u64)p;
  u32x4 s = {(u32)a, (u32)(a >> 32) & 0xffffu, bytes, 0x00020000u};
  return s;
}
DI void dma16(u32x4 srd, u32 voff, u32 soff, u32 ldsaddr) {
  asm volatile("s_mov_b32 m0, %0\n\ts_nop 0\n\tbuffer_load_dwordx4 %1, %2, %3 offen lds"
               :: "s"(ldsaddr), "v"(voff), "s"(srd), "s"(soff) : "memory");
}
DI void dma16_dev(u32x4 srd, u32 voff, u32 soff, u32 ldsaddr) {
  asm volatile("s_mov_b32 m0, %0\n\ts_nop 0\n\tbuffer_load_dwordx4 %1, %2, %3 offen sc1 lds"
               :: "s"(ldsaddr), "v"(voff), "s"(srd), "s"(soff) : "memory");
}
#define VMWAIT(n) asm volatile("s_waitcnt vmcnt(" #n ")" ::: "memory")
#define LGWAIT() asm volatile("s_waitcnt lgkmcnt(0)" ::: "memory")
#define BAR() asm volatile("s_waitcnt lgkmcnt(0)\n\ts_barrier" ::: "memory")

DI bf16x8 as_frag(u32x4 v) { return __builtin_bit_cast(bf16x8, v); }
DI bf16x8 lds_frag(u32 a) { return *(const LAS bf16x8*)a; }
DI bf16x8 tr_frag(u32 a0, u32 a1) {
  const bf16x4 lo = __builtin_amdgcn_ds_read_tr16_b64_v4bf16((LAS bf16x4*)a0);
  const bf16x4 hi = __builtin_amdgcn_ds_read_tr16_b64_v4bf16((LAS bf16x4*)a1);
  return __builtin_shufflevector(lo, hi, 0, 1, 2, 3, 4, 5, 6, 7);
}
DI u32 pk2(float a, float b) {
  const f32x2 v = {a, b};
  return __builtin_bit_cast(u32, __builtin_convertvector(v, bf16x2));
}
DI float bf_lo(u32 v) { return __uint_as_float(v << 16); }
DI float bf_hi(u32 v) { return __uint_as_float(v & 0xffff0000u); }
DI f32x4 mfma16(bf16x8 a, bf16x8 b, f32x4 c) { return __builtin_amdgcn_mfma_f32_16x16x32_bf16(a, b, c, 0, 0, 0); }
DI f32x16 mfma32(bf16x8 a, bf16x8 b, f32x16 c) { return __builtin_amdgcn_mfma_f32_32x32x16_bf16(a, b, c, 0, 0, 0); }
DI float sum32(float v) {
  const auto r = __builtin_amdgcn_permlane32_swap(__float_as_uint(v), __float_as_uint(v), false, false);
  return __uint_as_float(r[0]) + __uint_as_float(r[1]);
}
DI float sum16(float v) {
  const auto r = __builtin_amdgcn_permlane16_swap(__float_as_uint(v), __float_as_uint(v), false, false);
  return __uint_as_float(r[0]) + __uint_as_float(r[1]);
}
DI float max4g(float v) {
  auto r = __builtin_amdgcn_permlane32_swap(__float_as_uint(v), __float_as_uint(v), false, false);
  v = fmaxf(__uint_as_float(r[0]), __uint_as_float(r[1]));
  r = __builtin_amdgcn_permlane16_swap(__float_as_uint(v), __float_as_uint(v), false, false);
  return fmaxf(__uint_as_float(r[0]), __uint_as_float(r[1]));
}
DI float sum4g(float v) { return sum16(sum32(v)); }

DI void set_flag(void* ws, u32 off, u64 tag) {
  __hip_atomic_store((GAS u64*)((char*)ws + off), tag, __ATOMIC_RELAXED, __HIP_MEMORY_SCOPE_AGENT);
}
DI void wait_flags(void* ws, u32 off, u32 n, u32 lane, u64 tag) {
  GAS u64* p = (GAS u64*)((char*)ws + off) + (lane < n ? lane : 0u);
  #pragma unroll 1
  for (u32 spin = 0; spin < SPIN_CAP; ++spin) {
    const u64 v = __hip_atomic_load(p, __ATOMIC_RELAXED, __HIP_MEMORY_SCOPE_AGENT);
    if (__all(v == tag)) break;
    __builtin_amdgcn_s_sleep(1);
  }
}

DI void feature_map(const f32x4& t0, const f32x4& t1, bf16x8 wp0, bf16x8 wp1, float a, u32x4& pos, u32x4& neg) {
  const u32x4 tb = {pk2(t0[0], t0[1]), pk2(t0[2], t0[3]), pk2(t1[0], t1[1]), pk2(t1[2], t1[3])};
  const f32x4 z = {0.f, 0.f, 0.f, 0.f};
  const f32x4 p0 = mfma16(wp0, as_frag(tb), z), p1 = mfma16(wp1, as_frag(tb), z);
  const float p[7] = {p0[0], p0[1], p0[2], p0[3], p1[0], p1[1], p1[2]};
  float mx = p[0], mn = p[0];
#pragma unroll
  for (int i = 1; i < 7; ++i) { mx = fmaxf(mx, p[i]); mn = fminf(mn, p[i]); }
  mx = max4g(mx) * a;
  mn = -max4g(-mn) * a;
  float ep[7], en[7], sp = 0.f, sn = 0.f;
#pragma unroll
  for (int i = 0; i < 7; ++i) {
    ep[i] = __builtin_amdgcn_exp2f(__builtin_fmaf(p[i], a, -mx));
    en[i] = __builtin_amdgcn_exp2f(__builtin_fmaf(-p[i], a, mn));
    sp += ep[i]; sn += en[i];
  }
  const float rp = __builtin_amdgcn_rcpf(sum4g(sp)), rn = __builtin_amdgcn_rcpf(sum4g(sn));
  pos = u32x4{pk2(ep[0] * rp, ep[1] * rp), pk2(ep[2] * rp, ep[3] * rp), pk2(ep[4] * rp, ep[5] * rp), pk2(ep[6] * rp, 0.f)};
  neg = u32x4{pk2(en[0] * rn, en[1] * rn), pk2(en[2] * rn, en[3] * rn), pk2(en[4] * rn, en[5] * rn), pk2(en[6] * rn, 0.f)};
}

#define STAMP(i)

__global__ void __launch_bounds__(512, 2)
fused_block(const void* __restrict__ px, const void* __restrict__ pwqkv, const void* __restrict__ pwphi,
            const void* __restrict__ pwo, const void* __restrict__ pw1, const void* __restrict__ pb1,
            const void* __restrict__ pw2, void* __restrict__ pout, void* ws) {
  const u32 tid = threadIdx.x, lane = tid & 63;
  const u32 w = __builtin_amdgcn_readfirstlane(tid >> 6);
  const u32 xcd = blockIdx.x & 7, rr = blockIdx.x >> 3;
  const u32 T = 4 * (xcd >> 1) + (rr >> 3), hd = 8 * (xcd & 1) + (rr & 7);
  const u32 g = 4 * xcd + (rr >> 3), j = rr & 7;
  const u32 lds0 = (u32)(size_t)(LAS char*)g_lds;
  const u32 l15 = lane & 15, gq = lane >> 4, l31 = lane & 31, hh = lane >> 5;
  const u32 tq = l15 >> 2, tp = lane & 3;
  STAMP(0);

  const u32x4 sx = make_srd(px, 2048 * 896), sqkv = make_srd(pwqkv, 448 * 2688);
  const u32x4 sws = make_srd(ws, WS_END);
  const auto rws = __builtin_amdgcn_make_buffer_rsrc(ws, 0, WS_END, 0x00020000);

  u32x2 ep;
  asm volatile("buffer_load_dwordx2 %0, off, %1, 0 sc1" : "=v"(ep) : "s"(sws));
  {
    const u32x4 sphi = make_srd(pwphi, 16 * 1568);
    dma16(sphi, lane * 16, hd * 1568 + (w & 1) * 1024, lds0 + L_WPHI + (w & 1) * 1024);
  }
  const u32 wv_off = (lane >> 2) * 2688 + (((lane & 3) ^ (2 * ((lane >> 5) & 1))) << 4);
  const u32 wcol = (hd * 56) & ~15u;
  const u32 xv_off = (lane >> 3) * 896 + (((lane & 7) ^ ((lane >> 4) & 3)) << 4);
  const u32 xrow = (T * 128 + w * 16) * 896;
  const u32 xr = lds0 + L_XR + w * 8192;
  const u32 xv_off2 = (xv_off ^ 64) + 8 * 896;
#define W_SC(sc) do { \
    dma16(sqkv, wv_off, ((sc) * 8 + w) * 16 * 2688 + 896 + wcol, lds0 + L_WK + ((sc) * 8 + w) * 1024); \
    dma16(sqkv, wv_off, ((sc) * 8 + w) * 16 * 2688 + 1792 + wcol, lds0 + L_WV + ((sc) * 8 + w) * 1024); } while (0)
#define X_CH(c) do { \
    dma16(sx, xv_off, xrow + (c) * 128, xr + ((c) & 3) * 2048); \
    dma16(sx, xv_off2, xrow + (c) * 128, xr + ((c) & 3) * 2048 + 1024); } while (0)
  W_SC(0); W_SC(1); X_CH(0); X_CH(1); W_SC(2); X_CH(2); X_CH(3);
  dma16(sqkv, wv_off, (24 + (w & 3)) * 16 * 2688 + 896 * (1 + (w >> 2)) + wcol, lds0 + (w >> 2) * L_WV + (24 + (w & 3)) * 1024);

  const u32 xa0 = xr + l15 * 128 + ((gq ^ (l15 >> 1)) << 4);
  u32 wa[2];
#pragma unroll
  for (int ct = 0; ct < 2; ++ct) {
    const u32 byte = (8 * (hd & 1) + 32 * ct + 8 * tp) & 63;
    wa[ct] = lds0 + (8 * gq + tq) * 64 + (((byte >> 4) ^ (2 * (gq & 1))) << 4) + (byte & 15);
  }
  const bf16x8 ones = as_frag(u32x4{0x3f803f80u, 0x3f803f80u, 0x3f803f80u, 0x3f803f80u});
  const f32x4 z4 = {0.f, 0.f, 0.f, 0.f};
  f32x4 ak0 = z4, ak1 = z4, av0 = z4, av1 = z4, as1 = z4, as2 = z4, acs = z4;
  bf16x8 xf[14];
  const u32 csa = wa[w & 1] + ((w >> 1) & 1) * L_WV;
#define XREAD(s) xf[s] = lds_frag((xa0 ^ (((s) & 1) * 64)) + (((s) >> 1) & 3) * 2048)
#define KVSTEP(s) do { \
    ak0 = mfma16(tr_frag(wa[0] + L_WK + (s) * 2048, wa[0] + L_WK + (s) * 2048 + 256), xf[s], ak0); \
    ak1 = mfma16(tr_frag(wa[1] + L_WK + (s) * 2048, wa[1] + L_WK + (s) * 2048 + 256), xf[s], ak1); \
    av0 = mfma16(tr_frag(wa[0] + L_WV + (s) * 2048, wa[0] + L_WV + (s) * 2048 + 256), xf[s], av0); \
    av1 = mfma16(tr_frag(wa[1] + L_WV + (s) * 2048, wa[1] + L_WV + (s) * 2048 + 256), xf[s], av1); \
    as1 = mfma16(ones, xf[s], as1); as2 = mfma16(xf[s], xf[s], as2); } while (0)
#define CSSTEP(s) acs = mfma16(tr_frag(csa + (s) * 2048, csa + (s) * 2048 + 256), ones, acs)

  asm volatile("s_waitcnt vmcnt(7)" : "+v"(ep) :: "memory");
  BAR();
  STAMP(1);
  const u64 tag = (((u64)(u32)__builtin_amdgcn_readfirstlane(ep[1]) << 32) | (u32)__builtin_amdgcn_readfirstlane(ep[0])) + 1;
  XREAD(0); XREAD(1); XREAD(2); XREAD(3);
  LGWAIT();
  X_CH(4); X_CH(5);
  KVSTEP(0); KVSTEP(1); KVSTEP(2); KVSTEP(3);
  if (w < 4) { CSSTEP(0); CSSTEP(1); CSSTEP(2); CSSTEP(3); CSSTEP(4); CSSTEP(5); CSSTEP(6); CSSTEP(7); }
  bf16x8 wp[2];
#pragma unroll
  for (int ft = 0; ft < 2; ++ft) {
    const u32 i = 4 * ft + tp, f = 7 * tq + i;
    u32x4 v;
#pragma unroll
    for (int e = 0; e < 4; ++e) {
      const u32 d = 16 * (e >> 1) + 4 * gq + 2 * (e & 1);
      const bool ok = (i < 7) && (d < 28);
      const u32 a = lds0 + L_WPHI + (ok ? (d * 28 + f) * 2 : 0u);
      const u32 lo = *(const LAS unsigned short*)a, hi = *(const LAS unsigned short*)(a + 56);
      v[e] = ok ? (lo | (hi << 16)) : 0u;
    }
    wp[ft] = as_frag(v);
  }
  VMWAIT(5);
  BAR();
  XREAD(4); XREAD(5); XREAD(6); XREAD(7);
  LGWAIT();
  X_CH(6);
#pragma unroll
  for (int i = 0; i < 4; ++i) {
    const u32 u = min(w * 4 + i, 27u);
    dma16(sqkv, wv_off, u * 16 * 2688 + wcol, lds0 + L_WQ + u * 1024);
  }
  KVSTEP(4); KVSTEP(5); KVSTEP(6); KVSTEP(7);
  if (w < 4) { CSSTEP(8); CSSTEP(9); CSSTEP(10); CSSTEP(11); }
  VMWAIT(6);
  BAR();
  XREAD(8); XREAD(9); XREAD(10); XREAD(11);
  if (w < 4) {
    CSSTEP(12); CSSTEP(13);
    if (l15 == 0) *(LAS f32x4*)(lds0 + L_CS + w * 64 + gq * 16) = acs;
  }
  KVSTEP(8); KVSTEP(9); KVSTEP(10); KVSTEP(11);
  VMWAIT(4);
  BAR();
  STAMP(2);
  XREAD(12); XREAD(13);
  KVSTEP(12); KVSTEP(13);

  const float mean = as1[0] * (1.f / 448.f);
  float rstd;
  {
    float dg = (tp == 0) ? as2[0] : (tp == 1) ? as2[1] : (tp == 2) ? as2[2] : as2[3];
    dg = sum4g(gq == tq ? dg : 0.f);
    rstd = __builtin_amdgcn_rsqf(fmaxf(dg * (1.f / 448.f) - mean * mean, 0.f) + 1e-5f);
  }
  const float sca = rstd * LOG2E;
  {
    const f32x4 c0 = *(const LAS f32x4*)(lds0 + L_CS + gq * 16), c1 = *(const LAS f32x4*)(lds0 + L_CS + 64 + gq * 16);
    const f32x4 c2 = *(const LAS f32x4*)(lds0 + L_CS + 128 + gq * 16), c3 = *(const LAS f32x4*)(lds0 + L_CS + 192 + gq * 16);
    u32x4 pos, neg;
    feature_map(ak0 - mean * c0, ak1 - mean * c1, wp[0], wp[1], sca, pos, neg);
    const f32x4 v0 = (av0 - mean * c2) * rstd;
    f32x4 v1 = (av1 - mean * c3) * rstd;
    if (gq == 3) v1 = f32x4{1.f, 0.f, 0.f, 0.f};
    const u32 row = xr + l15 * PV_STRIDE + gq * 8;
    *(LAS u32x2*)(row) = u32x2{pos[0], pos[1]};
    *(LAS u32x2*)(row + 32) = u32x2{pos[2], pos[3]};
    *(LAS u32x2*)(row + 64) = u32x2{neg[0], neg[1]};
    *(LAS u32x2*)(row + 96) = u32x2{neg[2], neg[3]};
    *(LAS u32x2*)(row + 128) = u32x2{pk2(v0[0], v0[1]), pk2(v0[2], v0[3])};
    *(LAS u32x2*)(row + 160) = u32x2{pk2(v1[0], v1[1]), pk2(v1[2], v1[3])};
  }
  VMWAIT(0);
  BAR();
  STAMP(3);

  f32x4 aq0 = z4, aq1 = z4;
#define QPASS() do { _Pragma("unroll") for (int s = 0; s < 14; ++s) { \
    aq0 = mfma16(tr_frag(wa[0] + L_WQ + s * 2048, wa[0] + L_WQ + s * 2048 + 256), xf[s], aq0); \
    aq1 = mfma16(tr_frag(wa[1] + L_WQ + s * 2048, wa[1] + L_WQ + s * 2048 + 256), xf[s], aq1); } } while (0)
  if (w < 4) {
    const u32 base = lds0 + L_XR + (gq >> 1) * 8192 + (8 * (gq & 1) + tq) * PV_STRIDE + 8 * tp;
    const u32 aa = base + (w >> 1) * 64, ba = base + 128 + (w & 1) * 32;
    f32x4 d0 = z4, d1 = z4;
#pragma unroll
    for (int s = 0; s < 4; ++s) {
      const bf16x8 bv = tr_frag(ba + s * 16384, ba + s * 16384 + 4 * PV_STRIDE);
      d0 = mfma16(tr_frag(aa + s * 16384, aa + s * 16384 + 4 * PV_STRIDE), bv, d0);
      d1 = mfma16(tr_frag(aa + s * 16384 + 32, aa + s * 16384 + 32 + 4 * PV_STRIDE), bv, d1);
    }
    const u32x4 o = {pk2(d0[0], d0[1]), pk2(d0[2], d0[3]), pk2(d1[0], d1[1]), pk2(d1[2], d1[3])};
    __builtin_amdgcn_raw_buffer_store_b128(o, rws, lane * 16, WS_KVP + (hd * 16 + T) * 4096 + ((w & 1) * 2 + (w >> 1)) * 1024, 16);
  } else {
    if (w < 6) {
      f32x4 cq = z4;
      const u32 a = wa[w & 1] + L_WQ;
#pragma unroll
      for (int s = 0; s < 14; ++s) cq = mfma16(tr_frag(a + s * 2048, a + s * 2048 + 256), ones, cq);
      if (l15 == 0) *(LAS f32x4*)(lds0 + L_CS + w * 64 + gq * 16) = cq;
    }
    QPASS();
  }
  VMWAIT(0);
  BAR();
  if (tid == 0) set_flag(ws, WS_F1 + hd * 256 + T * 8, tag);
  STAMP(4);

  const bool act = j < 7;
  const u32 jc = act ? j : 0u;
  const u32x4 sw1 = make_srd(pw1, 448 * 3584), sw2 = make_srd(pw2, 1792 * 896), swo = make_srd(pwo, 448 * 896);
  const u32 r128_off = (lane >> 3) * 896 + (((lane & 7) ^ (4 * ((lane >> 4) & 1))) << 4);
  const u32 w1_off = (lane >> 2) * 3584 + (lane & 3) * 16;
  const u32 sa = lds0 + w * 8192, sb = lds0 + L_XR + w * 8192;
#define W1_ROUND(rd, dst) do { _Pragma("unroll") for (int i = 0; i < 7; ++i) \
    dma16(sw1, w1_off, ((rd) * 7 + i) * 16 * 3584 + jc * 512 + w * 64, (dst) + i * 1024); } while (0)
  bf16x8 w1f[28];
  const u32 w1a = (4 * hh + tq) * 64 + ((lane >> 4) & 1) * 32 + 8 * tp;
#define W1_READ(rd, src) do { _Pragma("unroll") for (int i = 0; i < 7; ++i) \
    w1f[(rd) * 7 + i] = tr_frag((src) + w1a + i * 1024, (src) + w1a + i * 1024 + 512); } while (0)
#pragma unroll
  for (int i = 0; i < 7; ++i) dma16(swo, r128_off, (w * 7 + i) * 8 * 896 + jc * 128, lds0 + (w * 7 + i) * 1024);
  W1_ROUND(0, sb);
  const u32 tt = w >> 2, kq = w & 3;
  u32x2 xres0, xres1;
  u32 b1v;
  {
    const auto rx = __builtin_amdgcn_make_buffer_rsrc((void*)px, 0, 2048 * 896, 0x00020000);
    const u32 o = (g * 64 + tt * 32 + l31) * 896 + (jc * 64 + kq * 16 + 4 * hh) * 2;
    xres0 = __builtin_amdgcn_raw_buffer_load_b64(rx, o, 0, 0);
    xres1 = __builtin_amdgcn_raw_buffer_load_b64(rx, o + 16, 0, 0);
    b1v = ((const unsigned short*)pb1)[jc * 256 + w * 32 + l31];
  }

  if (w < 4) QPASS();
  u32x4 pq_pos, pq_neg;
  {
    const f32x4 c0 = *(const LAS f32x4*)(lds0 + L_CS + 256 + gq * 16), c1 = *(const LAS f32x4*)(lds0 + L_CS + 320 + gq * 16);
    feature_map(aq0 - mean * c0, aq1 - mean * c1, wp[0], wp[1], sca, pq_pos, pq_neg);
  }
  asm volatile("s_waitcnt vmcnt(0)" : "+v"(xres0), "+v"(xres1), "+v"(b1v) :: "memory");
  W1_READ(0, sb);
  STAMP(5);
  if (w == 7) wait_flags(ws, WS_F1 + hd * 256, 16, lane, tag);
  BAR();
  STAMP(6);

  u32x4 pl[8];
#pragma unroll
  for (int i = 0; i < 8; ++i)
    pl[i] = __builtin_amdgcn_raw_buffer_load_b128(rws, gq * 4096 + l15 * 16,
                                                  WS_KVP + (hd * 16 + 4 * (i & 3)) * 4096 + (2 * w + (i >> 2)) * 256, 16);
  bf16x8 wof[8][2];
  {
    const u32 sw = ((lane >> 4) & 1) * 32 + 8 * tp;
    const u32 base = lds0 + kq * 14336 + (4 * hh + tq) * 128;
#pragma unroll
    for (int nt = 0; nt < 2; ++nt) {
      const u32 a = base + ((nt ^ (tq >> 1)) * 64) + sw;
      const u32 a2 = a + (hh ? 0u : 2048u);
#pragma unroll
      for (int h4 = 0; h4 < 4; ++h4) {
        wof[2 * h4][nt] = tr_frag(a + h4 * 3584, a + h4 * 3584 + 2048);
        wof[2 * h4 + 1][nt] = tr_frag(a + h4 * 3584 + 1024, a2 + h4 * 3584 + 1024);
      }
    }
  }
  {
    u32x4 sel;
#pragma unroll
    for (int e = 0; e < 4; ++e) sel[e] = (l15 == 2 * e) ? 0x00003f80u : (l15 == 2 * e + 1) ? 0x3f800000u : 0u;
#pragma unroll
    for (int b = 0; b < 2; ++b) {
      f32x4 d = z4;
#pragma unroll
      for (int i = 0; i < 4; ++i) d = mfma16(as_frag(sel), as_frag(pl[4 * b + i]), d);
      if (lane < 32) *(LAS u32x2*)(lds0 + L_KVS + (2 * w + b) * 256 + l15 * 16 + gq * 8) = u32x2{pk2(d[0], d[1]), pk2(d[2], d[3])};
    }
  }
  BAR();
  {
    const u32 a = lds0 + L_KVS + lane * 16;
    f32x4 o0 = mfma16(lds_frag(a), as_frag(pq_pos), z4), o1 = mfma16(lds_frag(a + 2048), as_frag(pq_pos), z4);
    o0 = mfma16(lds_frag(a + 1024), as_frag(pq_neg), o0);
    o1 = mfma16(lds_frag(a + 3072), as_frag(pq_neg), o1);
    const float rd = __builtin_amdgcn_rcpf(sum4g(gq == 3 ? o1[0] : 0.f) + 1e-6f);
    o0 *= rd; o1 *= rd;
    u32x4 o = {pk2(o0[0], o0[1]), pk2(o0[2], o0[3]), pk2(o1[0], o1[1]), pk2(o1[2], o1[3])};
    if (gq == 3) { o[2] = 0u; o[3] = 0u; }
    __builtin_amdgcn_raw_buffer_store_b128(o, rws, (gq >> 1) * 1024 + ((gq & 1) * 32 + (w & 1) * 16 + l15) * 16,
                                           WS_OB + (2 * T + (w >> 2)) * 65536 + ((w >> 1) & 1) * 32768 + hd * 2048, 16);
  }
  VMWAIT(0);
  BAR();
  if (tid == 0) set_flag(ws, WS_F2 + T * 256 + hd * 8, tag);
  STAMP(7);
  if (!act) return;

  W1_ROUND(1, sa); W1_ROUND(2, sb);
  VMWAIT(0);
  W1_READ(1, sa); W1_READ(2, sb);
  STAMP(8);
  if (w == 7) wait_flags(ws, WS_F2 + (g >> 1) * 256, 16, lane, tag);
  BAR();
  STAMP(9);
  if (blockIdx.x == 0 && tid == 0) set_flag(ws, WS_EPOCH, tag);

  u32 hres[4];
  {
    u32x4 of[8];
#pragma unroll
    for (int i = 0; i < 8; ++i)
      of[i] = __builtin_amdgcn_raw_buffer_load_b128(rws, lane * 16, WS_OB + g * 65536 + tt * 32768 + kq * 8192 + i * 1024, 16);
    f32x16 a0, a1;
#pragma unroll
    for (int i = 0; i < 16; ++i) { a0[i] = 0.f; a1[i] = 0.f; }
#pragma unroll
    for (int i = 0; i < 8; ++i) { a0 = mfma32(wof[i][0], as_frag(of[i]), a0); a1 = mfma32(wof[i][1], as_frag(of[i]), a1); }
    W1_ROUND(3, sa);
    float mine[8];
    const u32 red = lds0 + L_RED + tt * 32768 + lane * 16;
#pragma unroll
    for (int q = 0; q < 4; ++q) {
      const f32x16& a = (q >> 1) ? a1 : a0;
      const int o = 8 * (q & 1);
      if (kq == (u32)q) {
#pragma unroll
        for (int e = 0; e < 8; ++e) mine[e] = a[o + e];
      } else {
        *(LAS f32x4*)(red + q * 8192 + kq * 2048) = f32x4{a[o], a[o + 1], a[o + 2], a[o + 3]};
        *(LAS f32x4*)(red + q * 8192 + kq * 2048 + 1024) = f32x4{a[o + 4], a[o + 5], a[o + 6], a[o + 7]};
      }
    }
    BAR();
#pragma unroll
    for (int s = 1; s < 4; ++s) {
      const u32 src = red + kq * 8192 + ((kq + s) & 3) * 2048;
      const f32x4 u0 = *(const LAS f32x4*)src, u1 = *(const LAS f32x4*)(src + 1024);
#pragma unroll
      for (int e = 0; e < 4; ++e) { mine[e] += u0[e]; mine[4 + e] += u1[e]; }
    }
    mine[0] += bf_lo(xres0[0]); mine[1] += bf_hi(xres0[0]); mine[2] += bf_lo(xres0[1]); mine[3] += bf_hi(xres0[1]);
    mine[4] += bf_lo(xres1[0]); mine[5] += bf_hi(xres1[0]); mine[6] += bf_lo(xres1[1]); mine[7] += bf_hi(xres1[1]);
    float s1 = 0.f, s2 = 0.f;
#pragma unroll
    for (int e = 0; e < 4; ++e) hres[e] = pk2(mine[2 * e], mine[2 * e + 1]);
#pragma unroll
    for (int e = 0; e < 4; ++e) {
      const float lo = bf_lo(hres[e]), hi = bf_hi(hres[e]);
      s1 += lo + hi; s2 = __builtin_fmaf(lo, lo, __builtin_fmaf(hi, hi, s2));
    }
    __builtin_amdgcn_raw_buffer_store_b128(u32x4{hres[0], hres[1], hres[2], hres[3]}, rws, lane * 16,
                                           WS_HB + g * HB_STRIDE + (tt * 28 + 4 * j + kq) * 1024, 16);
    s1 = sum32(s1); s2 = sum32(s2);
    if (lane < 32)
      __builtin_amdgcn_raw_buffer_store_b64(u32x2{__float_as_uint(s1), __float_as_uint(s2)}, rws, lane * 8,
                                            WS_HB + g * HB_STRIDE + 57344 + (4 * j + kq) * 512 + tt * 256, 16);
  }
  VMWAIT(0);
  BAR();
  if (tid == 0) set_flag(ws, WS_F3 + g * 256 + j * 8, tag);
  STAMP(10);

  W1_READ(3, sa);
  LGWAIT();
#define W2_STAGE(i, slot) do { u32 m_ = j + (i); if (m_ >= 7) m_ -= 7; \
    _Pragma("unroll") for (int u = 0; u < 4; ++u) \
      dma16(sw2, r128_off, (m_ * 256 + w * 32 + u * 8) * 896 + j * 128, sa + (slot) * 4096 + u * 1024); } while (0)
  bf16x8 w2f[7][2][2];
  const u32 w2a0 = sa + (4 * hh + tq) * 128 + ((tq >> 1) * 64) + ((lane >> 4) & 1) * 32 + 8 * tp;
#define W2_READ(i, slot) do { _Pragma("unroll") for (int s = 0; s < 2; ++s) _Pragma("unroll") for (int nt = 0; nt < 2; ++nt) { \
    const u32 a_ = (w2a0 ^ (nt * 64)) + (slot) * 4096 + s * 2048; w2f[i][s][nt] = tr_frag(a_, a_ + 1024); } } while (0)
  W2_STAGE(0, 0); W2_STAGE(1, 1);
  bf16x8 aaug;
  {
    f32x16 c;
#pragma unroll
    for (int i = 0; i < 16; ++i) c[i] = 0.f;
#pragma unroll
    for (int ks = 0; ks < 28; ++ks) c = mfma32(ones, w1f[ks], c);
    const float cs = -c[0];
    const u32 chi = pk2(cs, cs);
    const u32 clo = pk2(cs - bf_lo(chi), 0.f);
    const u32x4 v = {chi, (clo & 0xffffu) | (b1v << 16), b1v, 0u};
    aaug = as_frag(hh ? u32x4{0u, 0u, 0u, 0u} : v);
  }
  VMWAIT(0);
  W2_READ(0, 0); W2_READ(1, 1);
  LGWAIT();
  STAMP(11);
  if (w == 7) wait_flags(ws, WS_F3 + g * 256, 7, lane, tag);
  BAR();
  STAMP(12);

#pragma unroll
  for (int i = 0; i < 9; ++i) {
    const u32 u = min(w * 9 + i, 69u);
    dma16_dev(sws, lane * 16, WS_HB + g * HB_STRIDE + u * 1024, lds0 + L_HT + u * 1024);
  }
  W2_STAGE(2, 0); W2_STAGE(3, 1);
  VMWAIT(8);
  BAR();
  STAMP(13);
  u32x4 own[2][2];
#pragma unroll
  for (int t = 0; t < 2; ++t) {
    f32x16 acc;
#pragma unroll
    for (int i = 0; i < 16; ++i) acc[i] = 0.f;
    float s1 = 0.f, s2 = 0.f;
#pragma unroll
    for (int i = 0; i < 14; ++i) {
      const f32x2 v = *(const LAS f32x2*)(lds0 + L_HST + (14 * hh + i) * 512 + t * 256 + l31 * 8);
      s1 += v[0]; s2 += v[1];
    }
#pragma unroll
    for (int ks = 0; ks < 28; ++ks) acc = mfma32(w1f[ks], lds_frag(lds0 + L_HT + (t * 28 + ks) * 1024 + lane * 16), acc);
    const float m2 = sum32(s1) * (1.f / 448.f);
    const float var = fmaxf(sum32(s2) * (1.f / 448.f) - m2 * m2, 0.f) + 1e-5f;
    const float rs = __builtin_amdgcn_rsqf(var), sd = var * rs;
    const u32 mhi = pk2(m2, m2), shi = pk2(sd, sd);
    const u32 lo2 = pk2(m2 - bf_lo(mhi), sd - bf_lo(shi));
    const u32x4 bv = {(mhi & 0xffffu) | (lo2 << 16), (mhi & 0xffffu) | (shi << 16), lo2 >> 16, 0u};
    acc = mfma32(aaug, as_frag(hh ? u32x4{0u, 0u, 0u, 0u} : bv), acc);
    if (t == 1) {
      VMWAIT(0);
      BAR();
      if (tid == 0) set_flag(ws, WS_F4 + g * 512 + j * 8, tag);
    }
    const float rl = rs * LOG2E;
    float m[16];
#pragma unroll
    for (int i = 0; i < 16; ++i) {
      const float e = __builtin_amdgcn_exp2f(fminf(acc[i] * rl, 40.f));
      const float u = e * (e + 2.f);
      m[i] = (acc[i] * rs) * u * __builtin_amdgcn_rcpf(u + 2.f);
    }
#pragma unroll
    for (int s = 0; s < 2; ++s) {
      own[t][s] = u32x4{pk2(m[8 * s], m[8 * s + 1]), pk2(m[8 * s + 2], m[8 * s + 3]), pk2(m[8 * s + 4], m[8 * s + 5]), pk2(m[8 * s + 6], m[8 * s + 7])};
      __builtin_amdgcn_raw_buffer_store_b128(own[t][s], rws, lane * 16,
                                             WS_ACT + g * ACT_STRIDE + t * 114688 + (j * 8 + w) * 2048 + s * 1024, 16);
    }
  }
  W2_READ(2, 0); W2_READ(3, 1);
  VMWAIT(0);
  BAR();
  if (tid == 0) set_flag(ws, WS_F4 + g * 512 + 256 + j * 8, tag);
  STAMP(14);
  W2_STAGE(4, 0); W2_STAGE(5, 1);
  {
    const u32 row = lds0 + L_HRES + (tt * 32 + l31) * 128;
    const u32 s0 = 4 * kq + hh;
    *(LAS u32x2*)(row + ((s0 ^ l15) << 3)) = u32x2{hres[0], hres[1]};
    *(LAS u32x2*)(row + (((s0 + 2) ^ l15) << 3)) = u32x2{hres[2], hres[3]};
  }
  VMWAIT(0);
  W2_READ(4, 0); W2_READ(5, 1);
  LGWAIT();
  W2_STAGE(6, 0);
  VMWAIT(0);
  W2_READ(6, 0);

  const auto rout = __builtin_amdgcn_make_buffer_rsrc(pout, 0, 2048 * 896, 0x00020000);
#pragma unroll
  for (int t = 0; t < 2; ++t) {
    if (w == 7) wait_flags(ws, WS_F4 + g * 512 + t * 256, 7, lane, tag);
    BAR();
    STAMP(15 + 2 * t);
    u32x4 af[6][2];
#pragma unroll
    for (int i = 1; i < 7; ++i) {
      u32 m_ = j + i; if (m_ >= 7) m_ -= 7;
#pragma unroll
      for (int s = 0; s < 2; ++s)
        af[i - 1][s] = __builtin_amdgcn_raw_buffer_load_b128(rws, lane * 16 + s * 1024,
                                                             WS_ACT + g * ACT_STRIDE + t * 114688 + (m_ * 8 + w) * 2048, 16);
    }
    f32x16 a0, a1;
#pragma unroll
    for (int i = 0; i < 16; ++i) { a0[i] = 0.f; a1[i] = 0.f; }
#pragma unroll
    for (int i = 0; i < 7; ++i)
#pragma unroll
      for (int s = 0; s < 2; ++s) {
        const bf16x8 b = as_frag(i ? af[i ? i - 1 : 0][s] : own[t][s]);
        a0 = mfma32(w2f[i][s][0], b, a0); a1 = mfma32(w2f[i][s][1], b, a1);
      }
    const u32 row = lds0 + L_RED2 + t * 32768 + w * 4096 + l31 * 128;
#pragma unroll
    for (int q = 0; q < 4; ++q) {
      *(LAS u32x2*)(row + (((2 * q + hh) ^ l15) << 3)) = u32x2{pk2(a0[4 * q], a0[4 * q + 1]), pk2(a0[4 * q + 2], a0[4 * q + 3])};
      *(LAS u32x2*)(row + (((8 + 2 * q + hh) ^ l15) << 3)) = u32x2{pk2(a1[4 * q], a1[4 * q + 1]), pk2(a1[4 * q + 2], a1[4 * q + 3])};
    }
    BAR();
    const u32 tok = 4 * w + gq;
    const u32 so = tok * 128 + ((l15 ^ (tok & 15)) << 3);
    const u32x2 hv = *(const LAS u32x2*)(lds0 + L_HRES + t * 4096 + so);
    float o[4] = {0.f, 0.f, 0.f, 0.f};
#pragma unroll
    for (int u = 0; u < 8; ++u) {
      const u32x2 v = *(const LAS u32x2*)(lds0 + L_RED2 + t * 32768 + u * 4096 + so);
      o[0] += bf_lo(v[0]); o[1] += bf_hi(v[0]); o[2] += bf_lo(v[1]); o[3] += bf_hi(v[1]);
    }
    o[0] += bf_lo(hv[0]); o[1] += bf_hi(hv[0]); o[2] += bf_lo(hv[1]); o[3] += bf_hi(hv[1]);
    __builtin_amdgcn_raw_buffer_store_b64(u32x2{pk2(o[0], o[1]), pk2(o[2], o[3])}, rout,
                                          (g * 64 + t * 32 + tok) * 896 + j * 128 + l15 * 8, 0, 0);
    STAMP(16 + 2 * t);
  }
}

extern "C" void kernel_launch(void* const* d_in, const int* in_sizes, int n_in, void* d_out, int out_size,
                              void* d_ws, size_t ws_size, hipStream_t stream) {
  fused_block<<<256, 512, 0, stream>>>(d_in[0], d_in[1], d_in[2], d_in[3], d_in[5], d_in[6], d_in[7], d_out, d_ws);
}
```

```cpp
#pragma clang diagnostic ignored "-Wint-to-pointer-cast"
#include <hip/hip_runtime.h>
#include <hip/hip_bf16.h>

typedef unsigned u32;
typedef unsigned long long u64;
typedef u32 u32x2 __attribute__((ext_vector_type(2)));
typedef u32 u32x4 __attribute__((ext_vector_type(4)));
typedef float f32x2 __attribute__((ext_vector_type(2)));
typedef float f32x4 __attribute__((ext_vector_type(4)));
typedef float f32x16 __attribute__((ext_vector_type(16)));
typedef __bf16 bf16x2 __attribute__((ext_vector_type(2)));
typedef __bf16 bf16x4 __attribute__((ext_vector_type(4)));
typedef __bf16 bf16x8 __attribute__((ext_vector_type(8)));
#define LAS __attribute__((address_space(3)))
#define GAS __attribute__((address_space(1)))
#define DI __device__ __forceinline__

constexpr u32 WS_EPOCH = 0;
constexpr u32 WS_F1 = 0x1000, WS_F2 = 0x2000, WS_F3 = 0x3000, WS_F4 = 0x5000;
constexpr u32 WS_KVP = 0x10000, WS_OB = 0x110000, WS_HB = 0x310000, WS_ACT = 0x550000, WS_END = 0xC50000;
constexpr u32 HB_STRIDE = 73728, ACT_STRIDE = 229376;
constexpr u32 L_WK = 0, L_WV = 28672, L_WQ = 57344, L_WPHI = 86016, L_CS = 88064, L_KVS = 89088, L_XR = 98304;
constexpr u32 L_RED = 0, L_HT = 98304, L_HST = L_HT + 57344, L_RED2 = 98304;
constexpr u32 LDS_BYTES = 163840;
constexpr u32 PV_STRIDE = 200;
constexpr float LOG2E = 1.4426950408889634f;
constexpr u32 SPIN_CAP = 200000;

__shared__ __attribute__((aligned(16))) char g_lds[LDS_BYTES];

DI u32x4 make_srd(const void* p, u32 bytes) {
  const u64 a = (u64)p;
  u32x4 s = {(u32)a, (u32)(a >> 32) & 0xffffu, bytes, 0x00020000u};
  return s;
}
template <int N> DI void dma_seq(u32x4 srd, u32 voff, u32 soff, u32 stride, u32 ldsaddr) {
  u32 t;
  asm volatile("s_mov_b32 m0, %1\n\ts_mov_b32 %0, %4\n\tbuffer_load_dwordx4 %2, %3, %0 offen lds\n\t"
               ".rept %c6\n\ts_addk_i32 m0, 0x400\n\ts_add_u32 %0, %0, %5\n\tbuffer_load_dwordx4 %2, %3, %0 offen lds\n\t.endr"
               : "=&s"(t) : "s"(ldsaddr), "v"(voff), "s"(srd), "s"(soff), "s"(stride), "i"(N - 1) : "memory", "scc");
}
template <int N> DI void dma_seq_dev(u32x4 srd, u32 voff, u32 soff, u32 stride, u32 ldsaddr) {
  u32 t;
  asm volatile("s_mov_b32 m0, %1\n\ts_mov_b32 %0, %4\n\tbuffer_load_dwordx4 %2, %3, %0 offen sc1 lds\n\t"
               ".rept %c6\n\ts_addk_i32 m0, 0x400\n\ts_add_u32 %0, %0, %5\n\tbuffer_load_dwordx4 %2, %3, %0 offen sc1 lds\n\t.endr"
               : "=&s"(t) : "s"(ldsaddr), "v"(voff), "s"(srd), "s"(soff), "s"(stride), "i"(N - 1) : "memory", "scc");
}
DI void dma_x(u32x4 srd, u32 voff0, u32 voff1, u32 soff, u32 ldsaddr) {
  asm volatile("s_mov_b32 m0, %0\n\ts_nop 0\n\tbuffer_load_dwordx4 %1, %3, %4 offen lds\n\t"
               "s_addk_i32 m0, 0x400\n\ts_nop 0\n\tbuffer_load_dwordx4 %2, %3, %4 offen lds"
               :: "s"(ldsaddr), "v"(voff0), "v"(voff1), "s"(srd), "s"(soff) : "memory", "scc");
}
template <int OFF> DI void ld_dev(u32x4& dst, u32x4 srd, u32 voff, u32 soff) {
  asm volatile("buffer_load_dwordx4 %0, %1, %2, %3 offen offset:%c4 sc1" : "=v"(dst) : "v"(voff), "s"(srd), "s"(soff), "i"(OFF) : "memory");
}
#define LDWAIT(n, x) asm volatile("s_waitcnt vmcnt(%c1)" : "+v"(x) : "i"(n) : "memory")
#define SB() __builtin_amdgcn_sched_barrier(0)
#define OPAQUE(x) asm volatile("" : "+v"(x))
#define RAWBAR() asm volatile("s_barrier" ::: "memory")
DI float dot_ones(bf16x8 f, float acc) {
  const u32x4 v = __builtin_bit_cast(u32x4, f);
  const bf16x2 one = __builtin_bit_cast(bf16x2, 0x3f803f80u);
#pragma unroll
  for (int e = 0; e < 4; ++e) {
    const u32 x = v[e];
    acc = __builtin_amdgcn_fdot2_f32_bf16(__builtin_bit_cast(bf16x2, x), one, acc, false);
  }
  return acc;
}
#define VMWAIT(n) asm volatile("s_waitcnt vmcnt(" #n ")" ::: "memory")
#define LGWAIT() asm volatile("s_waitcnt lgkmcnt(0)" ::: "memory")
#define BAR() asm volatile("s_waitcnt lgkmcnt(0)\n\ts_barrier" ::: "memory")

DI bf16x8 as_frag(u32x4 v) { return __builtin_bit_cast(bf16x8, v); }
DI bf16x8 lds_frag(u32 a) { return *(const LAS bf16x8*)a; }
DI bf16x8 tr_frag(u32 a0, u32 a1) {
  const bf16x4 lo = __builtin_amdgcn_ds_read_tr16_b64_v4bf16((LAS bf16x4*)a0);
  const bf16x4 hi = __builtin_amdgcn_ds_read_tr16_b64_v4bf16((LAS bf16x4*)a1);
  return __builtin_shufflevector(lo, hi, 0, 1, 2, 3, 4, 5, 6, 7);
}
DI u32 pk2(float a, float b) {
  const f32x2 v = {a, b};
  return __builtin_bit_cast(u32, __builtin_convertvector(v, bf16x2));
}
DI float bf_lo(u32 v) { return __uint_as_float(v << 16); }
DI float bf_hi(u32 v) { return __uint_as_float(v & 0xffff0000u); }
DI f32x4 mfma16(bf16x8 a, bf16x8 b, f32x4 c) { return __builtin_amdgcn_mfma_f32_16x16x32_bf16(a, b, c, 0, 0, 0); }
DI f32x16 mfma32(bf16x8 a, bf16x8 b, f32x16 c) { return __builtin_amdgcn_mfma_f32_32x32x16_bf16(a, b, c, 0, 0, 0); }
DI float sum32(float v) {
  const auto r = __builtin_amdgcn_permlane32_swap(__float_as_uint(v), __float_as_uint(v), false, false);
  return __uint_as_float(r[0]) + __uint_as_float(r[1]);
}
DI float sum16(float v) {
  const auto r = __builtin_amdgcn_permlane16_swap(__float_as_uint(v), __float_as_uint(v), false, false);
  return __uint_as_float(r[0]) + __uint_as_float(r[1]);
}
DI float max4g(float v) {
  auto r = __builtin_amdgcn_permlane32_swap(__float_as_uint(v), __float_as_uint(v), false, false);
  v = fmaxf(__uint_as_float(r[0]), __uint_as_float(r[1]));
  r = __builtin_amdgcn_permlane16_swap(__float_as_uint(v), __float_as_uint(v), false, false);
  return fmaxf(__uint_as_float(r[0]), __uint_as_float(r[1]));
}
DI float sum4g(float v) { return sum16(sum32(v)); }

DI void set_flag(void* ws, u32 off, u64 tag) {
  __hip_atomic_store((GAS u64*)((char*)ws + off), tag, __ATOMIC_RELAXED, __HIP_MEMORY_SCOPE_AGENT);
}
DI void wait_flags(void* ws, u32 off, u32 n, u32 lane, u64 tag) {
  GAS u64* p = (GAS u64*)((char*)ws + off) + (lane < n ? lane : 0u);
  #pragma unroll 1
  for (u32 spin = 0; spin < SPIN_CAP; ++spin) {
    const u64 v = __hip_atomic_load(p, __ATOMIC_RELAXED, __HIP_MEMORY_SCOPE_AGENT);
    if (__all(v == tag)) break;
    __builtin_amdgcn_s_sleep(1);
  }
}

DI void feature_map(const f32x4& t0, const f32x4& t1, bf16x8 wp0, bf16x8 wp1, float a, u32x4& pos, u32x4& neg) {
  const u32x4 tb = {pk2(t0[0], t0[1]), pk2(t0[2], t0[3]), pk2(t1[0], t1[1]), pk2(t1[2], t1[3])};
  const f32x4 z = {0.f, 0.f, 0.f, 0.f};
  const f32x4 p0 = mfma16(wp0, as_frag(tb), z), p1 = mfma16(wp1, as_frag(tb), z);
  const float p[7] = {p0[0], p0[1], p0[2], p0[3], p1[0], p1[1], p1[2]};
  float mx = p[0], mn = p[0];
#pragma unroll
  for (int i = 1; i < 7; ++i) { mx = fmaxf(mx, p[i]); mn = fminf(mn, p[i]); }
  mx = max4g(mx) * a;
  mn = -max4g(-mn) * a;
  float ep[7], en[7], sp = 0.f, sn = 0.f;
#pragma unroll
  for (int i = 0; i < 7; ++i) {
    ep[i] = __builtin_amdgcn_exp2f(__builtin_fmaf(p[i], a, -mx));
    en[i] = __builtin_amdgcn_exp2f(__builtin_fmaf(-p[i], a, mn));
    sp += ep[i]; sn += en[i];
  }
  const float rp = __builtin_amdgcn_rcpf(sum4g(sp)), rn = __builtin_amdgcn_rcpf(sum4g(sn));
  pos = u32x4{pk2(ep[0] * rp, ep[1] * rp), pk2(ep[2] * rp, ep[3] * rp), pk2(ep[4] * rp, ep[5] * rp), pk2(ep[6] * rp, 0.f)};
  neg = u32x4{pk2(en[0] * rn, en[1] * rn), pk2(en[2] * rn, en[3] * rn), pk2(en[4] * rn, en[5] * rn), pk2(en[6] * rn, 0.f)};
}

#define STAMP(i)
#define STAMP_OUT()

__global__ void __launch_bounds__(512, 2)
fused_block(const void* __restrict__ px, const void* __restrict__ pwqkv, const void* __restrict__ pwphi,
            const void* __restrict__ pwo, const void* __restrict__ pw1, const void* __restrict__ pb1,
            const void* __restrict__ pw2, void* __restrict__ pout, void* ws) {
  const u32 tid = threadIdx.x, lane = tid & 63;
  const u32 w = __builtin_amdgcn_readfirstlane(tid >> 6);
  const u32 xcd = blockIdx.x & 7, rr = blockIdx.x >> 3;
  const u32 T = 4 * (xcd >> 1) + (rr >> 3), hd = 8 * (xcd & 1) + (rr & 7);
  const u32 g = 4 * xcd + (rr >> 3), j = rr & 7;
  const u32 lds0 = (u32)(size_t)(LAS char*)g_lds;
  const u32 l15 = lane & 15, gq = lane >> 4, l31 = lane & 31, hh = lane >> 5;
  const u32 tq = l15 >> 2, tp = lane & 3;
  STAMP(0);
  u32 warm = 0;
  if (w == 7) {
    u64 pc; u32 len;
    asm volatile(".Lfb_start:\n\ts_getpc_b64 %0\n\ts_mov_b32 %1, .Lfb_end-.Lfb_start" : "=s"(pc), "=s"(len));
    const u32x4 sc = {(u32)pc, (u32)(pc >> 32) & 0xffffu, len, 0x00020000u};
    asm volatile("buffer_load_dword %0, %1, %2, %3 offen" : "=v"(warm) : "v"(lane * 64), "s"(sc), "s"((rr & 7) * 4096) : "memory");
  }

  const u32x4 sx = make_srd(px, 2048 * 896), sqkv = make_srd(pwqkv, 448 * 2688);
  const u32x4 sws = make_srd(ws, WS_END);
  const auto rws = __builtin_amdgcn_make_buffer_rsrc(ws, 0, WS_END, 0x00020000);

  u32x2 ep;
  asm volatile("buffer_load_dwordx2 %0, off, %1, 0 sc1" : "=v"(ep) : "s"(sws) : "memory");
  dma_seq<1>(make_srd(pwphi, 16 * 1568), lane * 16, hd * 1568 + (w & 1) * 1024, 0, lds0 + L_WPHI + (w & 1) * 1024);
  const u32 wv_off = (lane >> 2) * 2688 + (((lane & 3) ^ (2 * ((lane >> 5) & 1))) << 4);
  const u32 wcol = (hd * 56) & ~15u;
  const u32 xv_off = (lane >> 3) * 896 + (((lane & 7) ^ ((lane >> 4) & 3)) << 4);
  const u32 xv_off2 = (xv_off ^ 64) + 8 * 896;
  const u32 xrow = (T * 128 + w * 16) * 896;
  const u32 xr = lds0 + L_XR + w * 8192;
  const u32 wsrc = (w & 3) * 16 * 2688 + 896 * (1 + (w >> 2)) + wcol, wdst = lds0 + (w >> 2) * L_WV + (w & 3) * 1024;
#define W_CH(c) dma_seq<1>(sqkv, wv_off, wsrc + (c) * 64 * 2688, 0, wdst + (c) * 4096)
#define X_CH(c) dma_x(sx, xv_off, xv_off2, xrow + (c) * 128, xr + ((c) & 3) * 2048)
#define Q_CH(i) dma_seq<1>(sqkv, wv_off, min(w * 4 + (i), 27u) * 16 * 2688 + wcol, 0, lds0 + L_WQ + min(w * 4 + (i), 27u) * 1024)
  W_CH(0); W_CH(1); X_CH(0); W_CH(2); X_CH(1); W_CH(3); X_CH(2); W_CH(4); X_CH(3);

  u32 xa0 = xr + l15 * 128 + ((gq ^ (l15 >> 1)) << 4), xa1 = xa0 ^ 64;
  u32 wa[2];
#pragma unroll
  for (int ct = 0; ct < 2; ++ct) {
    const u32 byte = (8 * (hd & 1) + 32 * ct + 8 * tp) & 63;
    wa[ct] = lds0 + (8 * gq + tq) * 64 + (((byte >> 4) ^ (2 * (gq & 1))) << 4) + (byte & 15);
  }
  u32 csa = wa[w & 1] + ((w >> 1) & 1) * L_WV + (w >> 2) * 2048;
  OPAQUE(xa0); OPAQUE(xa1); OPAQUE(wa[0]); OPAQUE(wa[1]); OPAQUE(csa);
  const bf16x8 ones = as_frag(u32x4{0x3f803f80u, 0x3f803f80u, 0x3f803f80u, 0x3f803f80u});
  const f32x4 z4 = {0.f, 0.f, 0.f, 0.f};
  f32x4 ak0 = z4, ak1 = z4, av0 = z4, av1 = z4, as1 = z4, as2 = z4, acs = z4;
  bf16x8 xf[14], fa[4], fb[4], fc;
#define WFR(F, s) do { _Pragma("unroll") for (int m_ = 0; m_ < 4; ++m_) \
    F[m_] = tr_frag(wa[m_ & 1] + (m_ >> 1) * L_WV + (s) * 2048, wa[m_ & 1] + (m_ >> 1) * L_WV + (s) * 2048 + 256); } while (0)
#define KV(F, s) do { ak0 = mfma16(F[0], xf[s], ak0); ak1 = mfma16(F[1], xf[s], ak1); av0 = mfma16(F[2], xf[s], av0); \
    av1 = mfma16(F[3], xf[s], av1); as1 = mfma16(ones, xf[s], as1); as2 = mfma16(xf[s], xf[s], as2); } while (0)
#define CSR(c) fc = tr_frag(csa + (c) * 4096, csa + (c) * 4096 + 256)
#define STAGE(c, ISSUE) do { \
    xf[2 * (c)] = lds_frag(xa0 + ((c) & 3) * 2048); xf[2 * (c) + 1] = lds_frag(xa1 + ((c) & 3) * 2048); \
    RAWBAR(); \
    if ((c) == 0) { WFR(fa, 0); CSR(0); SB(); acs = mfma16(fc, ones, acs); } \
    WFR(fb, 2 * (c) + 1); SB(); \
    KV(fa, 2 * (c)); SB(); \
    asm volatile("" :: "v"(xf[2 * (c) + 1])); ISSUE; \
    if ((c) < 6) { CSR((c) + 1); WFR(fa, 2 * (c) + 2); } SB(); \
    KV(fb, 2 * (c) + 1); \
    if ((c) < 6) acs = mfma16(fc, ones, acs); \
    SB(); } while (0)

  asm volatile("s_waitcnt vmcnt(9)" : "+v"(ep), "+v"(warm) :: "memory");
  STAMP(1);
  STAGE(0, W_CH(5); X_CH(4));
  const u64 tag = (((u64)(u32)__builtin_amdgcn_readfirstlane(ep[1]) << 32) | (u32)__builtin_amdgcn_readfirstlane(ep[0])) + 1;
  VMWAIT(9);
  STAGE(1, W_CH(6); X_CH(5));
  bf16x8 wp[2];
#pragma unroll
  for (int ft = 0; ft < 2; ++ft) {
    const u32 i = 4 * ft + tp, f = 7 * tq + i;
    u32x4 v;
#pragma unroll
    for (int e = 0; e < 4; ++e) {
      const bool ok = (i < 7) && (e < 2 || gq < 3);
      const u32 a = lds0 + L_WPHI + (4 * gq * 28 + f) * 2 + (16 * (e >> 1) + 2 * (e & 1)) * 56;
      const u32 lo = *(const LAS unsigned short*)a, hi = *(const LAS unsigned short*)(a + 56);
      v[e] = ok ? (lo | (hi << 16)) : 0u;
    }
    wp[ft] = as_frag(v);
  }
  VMWAIT(9);
  STAGE(2, X_CH(6); Q_CH(0));
  VMWAIT(9);
  STAGE(3, Q_CH(1); Q_CH(2); Q_CH(3));
  VMWAIT(9);
  STAGE(4, );
  VMWAIT(6);
  STAGE(5, );
  if (l15 == 0) *(LAS f32x4*)(lds0 + L_CS + w * 64 + gq * 16) = acs;
  asm volatile("s_waitcnt vmcnt(4) lgkmcnt(0)" ::: "memory");
  STAMP(2);
  STAGE(6, );

  const float mean = as1[0] * (1.f / 448.f);
  float rstd;
  {
    float dg = (tp == 0) ? as2[0] : (tp == 1) ? as2[1] : (tp == 2) ? as2[2] : as2[3];
    dg = sum4g(gq == tq ? dg : 0.f);
    rstd = __builtin_amdgcn_rsqf(fmaxf(dg * (1.f / 448.f) - mean * mean, 0.f) + 1e-5f);
  }
  const float sca = rstd * LOG2E;
  {
    f32x4 c[4];
#pragma unroll
    for (int m = 0; m < 4; ++m)
      c[m] = *(const LAS f32x4*)(lds0 + L_CS + m * 64 + gq * 16) + *(const LAS f32x4*)(lds0 + L_CS + 256 + m * 64 + gq * 16);
    u32x4 pos, neg;
    feature_map(ak0 - mean * c[0], ak1 - mean * c[1], wp[0], wp[1], sca, pos, neg);
    const f32x4 v0 = (av0 - mean * c[2]) * rstd;
    f32x4 v1 = (av1 - mean * c[3]) * rstd;
    if (gq == 3) v1 = f32x4{1.f, 0.f, 0.f, 0.f};
    const u32 row = xr + l15 * PV_STRIDE + gq * 8;
    *(LAS u32x2*)(row) = u32x2{pos[0], pos[1]};
    *(LAS u32x2*)(row + 32) = u32x2{pos[2], pos[3]};
    *(LAS u32x2*)(row + 64) = u32x2{neg[0], neg[1]};
    *(LAS u32x2*)(row + 96) = u32x2{neg[2], neg[3]};
    *(LAS u32x2*)(row + 128) = u32x2{pk2(v0[0], v0[1]), pk2(v0[2], v0[3])};
    *(LAS u32x2*)(row + 160) = u32x2{pk2(v1[0], v1[1]), pk2(v1[2], v1[3])};
  }
  VMWAIT(0);
  BAR();
  STAMP(3);

  f32x4 aq0 = z4, aq1 = z4;
  u32 qa0 = wa[0] + L_WQ, qa1 = wa[1] + L_WQ;
  OPAQUE(qa0); OPAQUE(qa1);
#define QFR(F, s) do { F[0] = tr_frag(qa0 + (s) * 2048, qa0 + (s) * 2048 + 256); F[1] = tr_frag(qa1 + (s) * 2048, qa1 + (s) * 2048 + 256); \
    F[2] = tr_frag(qa0 + (s) * 2048 + 2048, qa0 + (s) * 2048 + 2304); F[3] = tr_frag(qa1 + (s) * 2048 + 2048, qa1 + (s) * 2048 + 2304); } while (0)
#define QM(F, s, CS) do { aq0 = mfma16(F[0], xf[s], aq0); aq1 = mfma16(F[1], xf[s], aq1); \
    aq0 = mfma16(F[2], xf[(s) + 1], aq0); aq1 = mfma16(F[3], xf[(s) + 1], aq1); \
    if (CS) { cq0 = mfma16(F[0], ones, cq0); cq1 = mfma16(F[1], ones, cq1); cq0 = mfma16(F[2], ones, cq0); cq1 = mfma16(F[3], ones, cq1); } } while (0)
#define QPASS(CS) do { QFR(fa, 0); \
    QFR(fb, 2); SB(); QM(fa, 0, CS && w == 4); SB(); QFR(fa, 4); SB(); QM(fb, 2, CS && w == 5); SB(); \
    QFR(fb, 6); SB(); QM(fa, 4, CS && w == 6); SB(); QFR(fa, 8); SB(); QM(fb, 6, CS && w == 7); SB(); \
    QFR(fb, 10); SB(); QM(fa, 8, CS && w == 4); SB(); QFR(fa, 12); SB(); QM(fb, 10, CS && w == 5); SB(); \
    QM(fa, 12, CS && w == 6); SB(); } while (0)
  if (w < 4) {
    u32 aa = lds0 + L_XR + (gq >> 1) * 8192 + (8 * (gq & 1) + tq) * PV_STRIDE + 8 * tp;
    u32 ba = aa + 128 + (w & 1) * 32;
    aa += (w >> 1) * 64;
    OPAQUE(aa); OPAQUE(ba);
    bf16x8 a0[4], a1[4], bv[4];
#pragma unroll
    for (int s = 0; s < 4; ++s) {
      bv[s] = tr_frag(ba + s * 16384, ba + s * 16384 + 4 * PV_STRIDE);
      a0[s] = tr_frag(aa + s * 16384, aa + s * 16384 + 4 * PV_STRIDE);
      a1[s] = tr_frag(aa + s * 16384 + 32, aa + s * 16384 + 32 + 4 * PV_STRIDE);
    }
    SB();
    f32x4 d0 = z4, d1 = z4;
#pragma unroll
    for (int s = 0; s < 4; ++s) { d0 = mfma16(a0[s], bv[s], d0); d1 = mfma16(a1[s], bv[s], d1); }
    const u32x4 o = {pk2(d0[0], d0[1]), pk2(d0[2], d0[3]), pk2(d1[0], d1[1]), pk2(d1[2], d1[3])};
    __builtin_amdgcn_raw_buffer_store_b128(o, rws, lane * 16, WS_KVP + (hd * 16 + T) * 4096 + ((w & 1) * 2 + (w >> 1)) * 1024, 16);
  } else {
    f32x4 cq0 = z4, cq1 = z4;
    QPASS(true);
    if (l15 == 0) {
      *(LAS f32x4*)(lds0 + L_CS + 512 + (w & 3) * 128 + gq * 16) = cq0;
      *(LAS f32x4*)(lds0 + L_CS + 512 + (w & 3) * 128 + 64 + gq * 16) = cq1;
    }
  }
  VMWAIT(0);
  BAR();
  if (tid == 0) set_flag(ws, WS_F1 + hd * 256 + T * 8, tag);
  STAMP(4);

  const bool act = j < 7;
  const u32 jc = act ? j : 0u;
  const u32x4 sw1 = make_srd(pw1, 448 * 3584), sw2 = make_srd(pw2, 1792 * 896);
  const u32 r128_off = (lane >> 3) * 896 + (((lane & 7) ^ (4 * ((lane >> 4) & 1))) << 4);
  const u32 w1_off = (lane >> 2) * 3584 + (lane & 3) * 16;
  const u32 w1src = jc * 512 + w * 64;
#define W1_DMA(k0, n, slot) dma_seq<n>(sw1, w1_off, w1src + (k0) * 16 * 3584, 16 * 3584, xr + (slot) * 1024)
  bf16x8 w1f[28];
  u32 w1a = xr + (4 * hh + tq) * 64 + (gq & 1) * 32 + 8 * tp;
  OPAQUE(w1a);
#define W1_READ(k0, n, slot) do { _Pragma("unroll") for (int i = 0; i < (n); ++i) \
    w1f[(k0) + i] = tr_frag(w1a + ((slot) + i) * 1024, w1a + ((slot) + i) * 1024 + 512); } while (0)
  float cs1a = 0.f, cs1b = 0.f;
#define W1_SUM(k0, n) do { _Pragma("unroll") for (int i = 0; i < (n); i += 2) { \
    cs1a = dot_ones(w1f[(k0) + i], cs1a); cs1b = dot_ones(w1f[(k0) + i + 1], cs1b); } } while (0)
  const u32 tt = w >> 2, kq = w & 3;
  if (w < 4) { f32x4 cq0, cq1; QPASS(false); }
  dma_seq<7>(make_srd(pwo, 448 * 896), r128_off, w * 56 * 896 + jc * 128, 8 * 896, lds0 + w * 7168);
  W1_DMA(0, 4, 0);
  u32x2 xres0, xres1;
  u32 b1v;
  {
    const u32 o = (g * 64 + tt * 32 + l31) * 896 + (jc * 64 + kq * 16 + 4 * hh) * 2;
    asm volatile("buffer_load_dwordx2 %0, %3, %4, 0 offen\n\tbuffer_load_dwordx2 %1, %3, %4, 0 offen offset:16\n\t"
                 "buffer_load_ushort %2, %5, %6, 0 offen"
                 : "=&v"(xres0), "=&v"(xres1), "=&v"(b1v)
                 : "v"(o), "s"(sx), "v"((jc * 256 + w * 32 + l31) * 2), "s"(make_srd(pb1, 3584)) : "memory");
  }
  u32x4 pq_pos, pq_neg;
  {
    f32x4 c0 = z4, c1 = z4;
#pragma unroll
    for (int m = 0; m < 4; ++m) {
      c0 += *(const LAS f32x4*)(lds0 + L_CS + 512 + m * 128 + gq * 16);
      c1 += *(const LAS f32x4*)(lds0 + L_CS + 512 + m * 128 + 64 + gq * 16);
    }
    feature_map(aq0 - mean * c0, aq1 - mean * c1, wp[0], wp[1], sca, pq_pos, pq_neg);
  }
  asm volatile("s_waitcnt vmcnt(0)" : "+v"(xres0), "+v"(xres1), "+v"(b1v) :: "memory");
  STAMP(5);
  if (w == 7) wait_flags(ws, WS_F1 + hd * 256, 16, lane, tag);
  BAR();
  STAMP(6);

  bf16x8 wof[8][2];
  {
    u32x4 pl[8];
    const u32 pv = gq * 4096 + l15 * 16, ps = WS_KVP + hd * 65536 + w * 512;
#pragma unroll
    for (int i = 0; i < 4; ++i) { ld_dev<0>(pl[2 * i], sws, pv, ps + i * 16384); ld_dev<256>(pl[2 * i + 1], sws, pv, ps + i * 16384); }
    W1_DMA(4, 4, 4);
    const u32 base = lds0 + kq * 14336 + (4 * hh + tq) * 128 + (gq & 1) * 32 + 8 * tp;
#pragma unroll
    for (int nt = 0; nt < 2; ++nt) {
      u32 a = base + ((nt ^ (tq >> 1)) * 64);
      u32 a2 = a + (hh ? 0u : 2048u);
      OPAQUE(a); OPAQUE(a2);
#pragma unroll
      for (int h4 = 0; h4 < 4; ++h4) {
        wof[2 * h4][nt] = tr_frag(a + h4 * 3584, a + h4 * 3584 + 2048);
        wof[2 * h4 + 1][nt] = tr_frag(a + h4 * 3584 + 1024, a2 + h4 * 3584 + 1024);
      }
    }
    u32x4 sel;
#pragma unroll
    for (int e = 0; e < 4; ++e) sel[e] = (l15 == 2 * e) ? 0x00003f80u : (l15 == 2 * e + 1) ? 0x3f800000u : 0u;
    SB();
    f32x4 d0 = z4, d1 = z4;
#pragma unroll
    for (int i = 0; i < 4; ++i) {
      LDWAIT(11 - 2 * i, pl[2 * i]); d0 = mfma16(as_frag(sel), as_frag(pl[2 * i]), d0);
      LDWAIT(10 - 2 * i, pl[2 * i + 1]); d1 = mfma16(as_frag(sel), as_frag(pl[2 * i + 1]), d1);
    }
    if (lane < 32) {
      const u32 a = lds0 + L_KVS + w * 512 + l15 * 16 + gq * 8;
      *(LAS u32x2*)(a) = u32x2{pk2(d0[0], d0[1]), pk2(d0[2], d0[3])};
      *(LAS u32x2*)(a + 256) = u32x2{pk2(d1[0], d1[1]), pk2(d1[2], d1[3])};
    }
  }
  BAR();
  STAMP(7);
  {
    const u32 a = lds0 + L_KVS + lane * 16;
    f32x4 o0 = mfma16(lds_frag(a), as_frag(pq_pos), z4), o1 = mfma16(lds_frag(a + 2048), as_frag(pq_pos), z4);
    o0 = mfma16(lds_frag(a + 1024), as_frag(pq_neg), o0);
    o1 = mfma16(lds_frag(a + 3072), as_frag(pq_neg), o1);
    const float rd = __builtin_amdgcn_rcpf(sum4g(gq == 3 ? o1[0] : 0.f) + 1e-6f);
    o0 *= rd; o1 *= rd;
    u32x4 o = {pk2(o0[0], o0[1]), pk2(o0[2], o0[3]), pk2(o1[0], o1[1]), pk2(o1[2], o1[3])};
    if (gq == 3) { o[2] = 0u; o[3] = 0u; }
    __builtin_amdgcn_raw_buffer_store_b128(o, rws, (gq >> 1) * 1024 + ((gq & 1) * 32 + (w & 1) * 16 + l15) * 16,
                                           WS_OB + (2 * T + (w >> 2)) * 65536 + ((w >> 1) & 1) * 32768 + hd * 2048, 16);
  }
  VMWAIT(0);
  BAR();
  if (tid == 0) set_flag(ws, WS_F2 + T * 256 + hd * 8, tag);
  STAMP(8);
  if (act) {
  W1_READ(0, 8, 0);
  LGWAIT();
  W1_DMA(8, 8, 0);
  W1_SUM(0, 8);
  VMWAIT(0);
  W1_READ(8, 8, 0);
  STAMP(9);
  if (w == 7) wait_flags(ws, WS_F2 + (g >> 1) * 256, 16, lane, tag);
  BAR();
  STAMP(10);

  u32 hres[4];
  {
    u32x4 of[8];
    const u32 os = WS_OB + g * 65536 + tt * 32768 + kq * 8192;
    ld_dev<0>(of[0], sws, lane * 16, os); ld_dev<1024>(of[1], sws, lane * 16, os);
    ld_dev<2048>(of[2], sws, lane * 16, os); ld_dev<3072>(of[3], sws, lane * 16, os);
    ld_dev<0>(of[4], sws, lane * 16, os + 4096); ld_dev<1024>(of[5], sws, lane * 16, os + 4096);
    ld_dev<2048>(of[6], sws, lane * 16, os + 4096); ld_dev<3072>(of[7], sws, lane * 16, os + 4096);
    W1_DMA(16, 8, 0);
    f32x16 a0, a1;
#pragma unroll
    for (int i = 0; i < 16; ++i) { a0[i] = 0.f; a1[i] = 0.f; }
#pragma unroll
    for (int i = 0; i < 8; ++i) {
      LDWAIT(15 - i, of[i]);
      a0 = mfma32(wof[i][0], as_frag(of[i]), a0); a1 = mfma32(wof[i][1], as_frag(of[i]), a1);
    }
    u32 red = lds0 + L_RED + tt * 32768 + lane * 16;
    u32 wr = red + kq * 2048, rd = red + kq * 8192;
    OPAQUE(wr); OPAQUE(rd);
#pragma unroll
    for (int q = 0; q < 4; ++q) {
      const f32x16& a = (q >> 1) ? a1 : a0;
      const int o = 8 * (q & 1);
      *(LAS f32x4*)(wr + q * 8192) = f32x4{a[o], a[o + 1], a[o + 2], a[o + 3]};
      *(LAS f32x4*)(wr + q * 8192 + 1024) = f32x4{a[o + 4], a[o + 5], a[o + 6], a[o + 7]};
    }
    BAR();
    STAMP(11);
    f32x4 m0 = {bf_lo(xres0[0]), bf_hi(xres0[0]), bf_lo(xres0[1]), bf_hi(xres0[1])};
    f32x4 m1 = {bf_lo(xres1[0]), bf_hi(xres1[0]), bf_lo(xres1[1]), bf_hi(xres1[1])};
#pragma unroll
    for (int s = 0; s < 4; ++s) { m0 += *(const LAS f32x4*)(rd + s * 2048); m1 += *(const LAS f32x4*)(rd + s * 2048 + 1024); }
    hres[0] = pk2(m0[0], m0[1]); hres[1] = pk2(m0[2], m0[3]); hres[2] = pk2(m1[0], m1[1]); hres[3] = pk2(m1[2], m1[3]);
    __builtin_amdgcn_raw_buffer_store_b128(u32x4{hres[0], hres[1], hres[2], hres[3]}, rws, lane * 16,
                                           WS_HB + g * HB_STRIDE + (tt * 28 + 4 * j + kq) * 1024, 16);
    float s1 = 0.f, s2 = 0.f;
#pragma unroll
    for (int e = 0; e < 4; ++e) {
      const float lo = bf_lo(hres[e]), hi = bf_hi(hres[e]);
      s1 += lo + hi; s2 = __builtin_fmaf(lo, lo, __builtin_fmaf(hi, hi, s2));
    }
    s1 = sum32(s1); s2 = sum32(s2);
    if (lane < 32)
      __builtin_amdgcn_raw_buffer_store_b64(u32x2{__float_as_uint(s1), __float_as_uint(s2)}, rws, lane * 8,
                                            WS_HB + g * HB_STRIDE + 57344 + (4 * j + kq) * 512 + tt * 256, 16);
  }
  VMWAIT(0);
  BAR();
  if (tid == 0) set_flag(ws, WS_F3 + g * 256 + j * 8, tag);
  STAMP(12);

  W1_READ(16, 8, 0);
  LGWAIT();
  W1_DMA(24, 4, 0);
  const u32 w2s = lds0 + w * 12288;
#define MEMBER(i) (j + (i) >= 7 ? j + (i) - 7 : j + (i))
#define W2_DMA(i, slot) dma_seq<4>(sw2, r128_off, (MEMBER(i) * 256 + w * 32) * 896 + j * 128, 8 * 896, w2s + (slot) * 4096)
  bf16x8 w2f[7][2][2];
  u32 w2a0 = w2s + (4 * hh + tq) * 128 + ((tq >> 1) * 64) + (gq & 1) * 32 + 8 * tp, w2a1 = w2a0 ^ 64;
  OPAQUE(w2a0); OPAQUE(w2a1);
#define W2_READ(i, slot) do { _Pragma("unroll") for (int s = 0; s < 2; ++s) { \
    w2f[i][s][0] = tr_frag(w2a0 + (slot) * 4096 + s * 2048, w2a0 + (slot) * 4096 + s * 2048 + 1024); \
    w2f[i][s][1] = tr_frag(w2a1 + (slot) * 4096 + s * 2048, w2a1 + (slot) * 4096 + s * 2048 + 1024); } } while (0)
  W2_DMA(0, 0); W2_DMA(1, 1);
  W1_SUM(8, 16);
  VMWAIT(8);
  W1_READ(24, 4, 0);
  VMWAIT(0);
  W2_READ(0, 0); W2_READ(1, 1);
  STAMP(13);
  if (w == 7) wait_flags(ws, WS_F3 + g * 256, 7, lane, tag);
  BAR();
  STAMP(14);

  bf16x8 aaug;
  {
    u32x2 sp[4];
    const u32 pg = lane >> 3;
    const u32 so = 57344 + pg * 512 + (w * 8 + (lane & 7)) * 8;
    asm volatile("buffer_load_dwordx2 %0, %4, %6, %7 offen sc1\n\tbuffer_load_dwordx2 %1, %4, %6, %8 offen sc1\n\t"
                 "buffer_load_dwordx2 %2, %4, %6, %9 offen sc1\n\tbuffer_load_dwordx2 %3, %5, %6, %7 offen sc1"
                 : "=&v"(sp[0]), "=&v"(sp[1]), "=&v"(sp[2]), "=&v"(sp[3])
                 : "v"(so), "v"(so + (pg < 4 ? 12288u : 0u)), "s"(sws), "s"(WS_HB + g * HB_STRIDE),
                   "s"(WS_HB + g * HB_STRIDE + 4096), "s"(WS_HB + g * HB_STRIDE + 8192) : "memory");
    dma_seq_dev<7>(sws, lane * 16, WS_HB + g * HB_STRIDE + w * 7168, 1024, lds0 + L_HT + w * 7168);
    W2_DMA(2, 0); W2_DMA(3, 1); W2_DMA(4, 2);
    W1_SUM(24, 4);
    const float cs = -sum32(cs1a + cs1b);
    const u32 chi = pk2(cs, cs);
    const u32 clo = pk2(cs - bf_lo(chi), 0.f);
    const u32x4 v = {chi, (clo & 0xffffu) | (b1v << 16), b1v, 0u};
    aaug = as_frag(hh ? u32x4{0u, 0u, 0u, 0u} : v);
    asm volatile("s_waitcnt vmcnt(19)" : "+v"(sp[0]), "+v"(sp[1]), "+v"(sp[2]), "+v"(sp[3]) :: "memory");
    float s1 = __uint_as_float(sp[0][0]) + __uint_as_float(sp[1][0]) + __uint_as_float(sp[2][0]) + (pg < 4 ? __uint_as_float(sp[3][0]) : 0.f);
    float s2 = __uint_as_float(sp[0][1]) + __uint_as_float(sp[1][1]) + __uint_as_float(sp[2][1]) + (pg < 4 ? __uint_as_float(sp[3][1]) : 0.f);
    s1 += __uint_as_float(__builtin_amdgcn_update_dpp(0u, __float_as_uint(s1), 0x128, 0xf, 0xf, false));
    s2 += __uint_as_float(__builtin_amdgcn_update_dpp(0u, __float_as_uint(s2), 0x128, 0xf, 0xf, false));
    const float m2 = sum4g(s1) * (1.f / 448.f);
    const float var = fmaxf(sum4g(s2) * (1.f / 448.f) - m2 * m2, 0.f) + 1e-5f;
    const float rs = __builtin_amdgcn_rsqf(var), sd = var * rs;
    const u32 mhi = pk2(m2, m2), shi = pk2(sd, sd);
    const u32 lo2 = pk2(m2 - bf_lo(mhi), sd - bf_lo(shi));
    if (lane < 8)
      *(LAS u32x4*)(lds0 + L_HST + (w * 8 + lane) * 16) =
          u32x4{(mhi & 0xffffu) | (lo2 << 16), (mhi & 0xffffu) | (shi << 16), lo2 >> 16, __float_as_uint(rs)};
  }
  VMWAIT(12);
  BAR();
  STAMP(15);
  u32x4 own[2][2];
  u32 hb = lds0 + L_HT + lane * 16;
  OPAQUE(hb);
  const u32 act_s = WS_ACT + g * ACT_STRIDE + (j * 8 + w) * 2048;
#define MISH2(dst, A, i) do { float m_[2]; _Pragma("unroll") for (int e_ = 0; e_ < 2; ++e_) { \
      const float ex_ = __builtin_amdgcn_exp2f(fminf(A[(i) + e_] * rl, 40.f)); const float u_ = ex_ * (ex_ + 2.f); \
      m_[e_] = (A[(i) + e_] * rs) * u_ * __builtin_amdgcn_rcpf(u_ + 2.f); } \
    dst = pk2(m_[0], m_[1]); } while (0)
  f32x16 accA, accB;
#pragma unroll
  for (int i = 0; i < 16; ++i) { accA[i] = 0.f; accB[i] = 0.f; }
  {
    bf16x8 hf[4];
    u32x4 sA = *(const LAS u32x4*)(lds0 + L_HST + l31 * 16), sB = *(const LAS u32x4*)(lds0 + L_HST + 512 + l31 * 16);
    hf[0] = lds_frag(hb); hf[1] = lds_frag(hb + 1024); hf[2] = lds_frag(hb + 2048);
#pragma unroll
    for (int ks = 0; ks < 28; ++ks) {
      hf[(ks + 3) & 3] = lds_frag(hb + (ks + 3) * 1024);
      SB();
      accA = mfma32(w1f[ks], hf[ks & 3], accA);
      SB();
    }
    float rs = __uint_as_float(sA[3]), rl = rs * LOG2E;
    sA[3] = 0u;
    accA = mfma32(aaug, as_frag(hh ? u32x4{0u, 0u, 0u, 0u} : sA), accA);
    SB();
#pragma unroll
    for (int ks = 0; ks < 28; ++ks) {
      if (ks < 25) hf[(ks + 31) & 3] = lds_frag(hb + (ks + 31) * 1024);
      SB();
      accB = mfma32(w1f[ks], hf[(ks + 28) & 3], accB);
      if (ks >= 1 && ks <= 8) MISH2(own[0][(ks - 1) >> 2][(ks - 1) & 3], accA, 2 * (ks - 1));
      if (ks == 5 || ks == 9)
        __builtin_amdgcn_raw_buffer_store_b128(own[0][ks == 9], rws, lane * 16 + (ks == 9) * 1024, act_s, 16);
      if (ks == 10) { VMWAIT(2); W2_READ(2, 0); }
      if (ks == 12) { asm volatile("" :: "v"(w2f[2][1][1])); W2_DMA(5, 0); }
      if (ks == 14) W2_READ(3, 1);
      if (ks == 16) { asm volatile("" :: "v"(w2f[3][1][1])); W2_DMA(6, 1); }
      if (ks == 18) W2_READ(4, 2);
      SB();
    }
    rs = __uint_as_float(sB[3]); rl = rs * LOG2E;
    sB[3] = 0u;
    accB = mfma32(aaug, as_frag(hh ? u32x4{0u, 0u, 0u, 0u} : sB), accB);
    VMWAIT(8);
    RAWBAR();
    if (tid == 0) set_flag(ws, WS_F4 + g * 512 + j * 8, tag);
    STAMP(16);
#pragma unroll
    for (int i = 0; i < 8; ++i) {
      MISH2(own[1][i >> 2][i & 3], accB, 2 * i);
      if ((i & 3) == 3)
        __builtin_amdgcn_raw_buffer_store_b128(own[1][i >> 2], rws, lane * 16 + (i >> 2) * 1024, act_s + 114688, 16);
    }
  }
  VMWAIT(0);
  BAR();
  if (tid == 0) set_flag(ws, WS_F4 + g * 512 + 256 + j * 8, tag);
  STAMP(17);
  W2_READ(5, 0); W2_READ(6, 1);
  {
    const u32 tok = tt * 32 + l31;
    const u32 row = lds0 + (tok >> 3) * 12288 + 8192 + (tok & 7) * 128;
    const u32 s0 = 4 * kq + hh;
    *(LAS u32x2*)(row + ((s0 ^ l15) << 3)) = u32x2{hres[0], hres[1]};
    *(LAS u32x2*)(row + (((s0 + 2) ^ l15) << 3)) = u32x2{hres[2], hres[3]};
  }

  {
    const auto rout = __builtin_amdgcn_make_buffer_rsrc(pout, 0, 2048 * 896, 0x00020000);
    const u32 as0 = WS_ACT + g * ACT_STRIDE + w * 2048;
    u32x4 af[6][2];
    f32x16 a0, a1;
#define ZERO_ACC() do { _Pragma("unroll") for (int i = 0; i < 16; ++i) { a0[i] = 0.f; a1[i] = 0.f; } } while (0)
#define P4M(i, s, B) do { a0 = mfma32(w2f[i][s][0], as_frag(B), a0); a1 = mfma32(w2f[i][s][1], as_frag(B), a1); } while (0)
#define RED_OUT(t) do { u32 row = lds0 + L_RED2 + (t) * 32768 + w * 4096 + l31 * 128; \
    _Pragma("unroll") for (int q = 0; q < 4; ++q) { \
      *(LAS u32x2*)(row + (((2 * q + hh) ^ l15) << 3)) = u32x2{pk2(a0[4 * q], a0[4 * q + 1]), pk2(a0[4 * q + 2], a0[4 * q + 3])}; \
      *(LAS u32x2*)(row + (((8 + 2 * q + hh) ^ l15) << 3)) = u32x2{pk2(a1[4 * q], a1[4 * q + 1]), pk2(a1[4 * q + 2], a1[4 * q + 3])}; } } while (0)
#define FINAL(t) do { const u32 tok = 4 * w + gq, sw_ = (l15 ^ (tok & 15)) << 3; \
    u32 so = lds0 + L_RED2 + (t) * 32768 + tok * 128 + sw_; OPAQUE(so); \
    const u32x2 hv = *(const LAS u32x2*)(lds0 + ((t) * 4 + (w >> 1)) * 12288 + 8192 + (tok & 7) * 128 + sw_); \
    float o[4] = {bf_lo(hv[0]), bf_hi(hv[0]), bf_lo(hv[1]), bf_hi(hv[1])}; \
    _Pragma("unroll") for (int u = 0; u < 8; ++u) { const u32x2 v = *(const LAS u32x2*)(so + u * 4096); \
      o[0] += bf_lo(v[0]); o[1] += bf_hi(v[0]); o[2] += bf_lo(v[1]); o[3] += bf_hi(v[1]); } \
    __builtin_amdgcn_raw_buffer_store_b64(u32x2{pk2(o[0], o[1]), pk2(o[2], o[3])}, rout, \
                                          (g * 64 + (t) * 32 + tok) * 896 + j * 128 + l15 * 8, 0, 0); } while (0)
    if (w == 7) wait_flags(ws, WS_F4 + g * 512, 7, lane, tag);
    BAR();
    STAMP(18);
#pragma unroll
    for (int i = 1; i < 7; ++i) {
      ld_dev<0>(af[i - 1][0], sws, lane * 16, as0 + MEMBER(i) * 16384);
      ld_dev<1024>(af[i - 1][1], sws, lane * 16, as0 + MEMBER(i) * 16384);
    }
    ZERO_ACC();
    P4M(0, 0, own[0][0]); P4M(0, 1, own[0][1]);
    if (w == 7) wait_flags(ws, WS_F4 + g * 512 + 256, 7, lane, tag);
    RAWBAR();
    STAMP(19);
#pragma unroll
    for (int i = 1; i < 7; ++i) {
      u32x4 c0 = af[i - 1][0], c1 = af[i - 1][1];
      asm volatile("s_waitcnt vmcnt(10)" : "+v"(c0), "+v"(c1) :: "memory");
      P4M(i, 0, c0); P4M(i, 1, c1);
      SB();
      ld_dev<0>(af[i - 1][0], sws, lane * 16, as0 + 114688 + MEMBER(i) * 16384);
      ld_dev<1024>(af[i - 1][1], sws, lane * 16, as0 + 114688 + MEMBER(i) * 16384);
      SB();
    }
    RED_OUT(0);
    ZERO_ACC();
    P4M(0, 0, own[1][0]); P4M(0, 1, own[1][1]);
    BAR();
    STAMP(20);
    FINAL(0);
    SB();
#pragma unroll
    for (int i = 1; i < 7; ++i) {
      asm volatile("s_waitcnt vmcnt(%c2)" : "+v"(af[i - 1][0]), "+v"(af[i - 1][1]) : "i"(13 - 2 * i) : "memory");
      P4M(i, 0, af[i - 1][0]); P4M(i, 1, af[i - 1][1]);
      SB();
    }
    RED_OUT(1);
    BAR();
    STAMP(21);
    FINAL(1);
    STAMP(22);
  }
  if (blockIdx.x == 0 && tid == 0) set_flag(ws, WS_EPOCH, tag);
  }
  STAMP_OUT();
  asm volatile(".Lfb_end:");
}

extern "C" void kernel_launch(void* const* d_in, const int* in_sizes, int n_in, void* d_out, int out_size,
                              void* d_ws, size_t ws_size, hipStream_t stream) {
  fused_block<<<256, 512, 0, stream>>>(d_in[0], d_in[1], d_in[2], d_in[3], d_in[5], d_in[6], d_in[7], d_out, d_ws);
}
```

```cpp
#include <hip/hip_runtime.h>

typedef unsigned u32;
constexpr u32 WS_END = 0xC50000;

__shared__ __attribute__((aligned(16))) char g_lds[163840];

__global__ void __launch_bounds__(512, 2)
fused_block(const void* x, u32, u32, const void* wqkv, u32, u32, void* ws, u32, u32, const void* wphi, u32, u32,
            const void* w1, u32, u32, const void* w2, u32, u32, const void* wo, u32, u32, const void* b1, u32, u32,
            void* out, u32, u32) {
  asm volatile(
    ".Lfb_start:\n"
    "s_load_dwordx16 s[4:19], %0, 0x0\n"
    "s_load_dwordx16 s[20:35], %0, 0x40\n"
    "s_load_dwordx4 s[36:39], %0, 0x80\n"
    "s_mov_b32 s65, %1\n"
    "v_mov_b32 v0, %2\n"
    "v_lshrrev_b32 v2, 6, v0\n"
    "v_and_b32 v196, 63, v0\n"
    "v_readfirstlane_b32 s40, v2\n"
    "s_cmp_lg_u32 s40, 3\n"
    "s_cbranch_scc1 .Lfb_cp1\n"
    "s_getpc_b64 s[56:57]\n"
    "s_mov_b32 s58, .Lfb_end-.Lfb_start-64\n"
    "s_mov_b32 s59, 0x20000\n"
    "s_bfe_u32 s60, s65, 0x30003\n"
    "s_lshl_b32 s60, s60, 12\n"
    "v_lshlrev_b32 v3, 6, v196\n"
    "buffer_load_dword v216, v3, s[56:59], s60 offen\n"
    ".Lfb_cp1:\n"
    "s_cmp_lg_u32 s40, 0\n"
    "s_cbranch_scc1 .Lfb_tw2\n"
    "v_lshlrev_b32 v243, 15, v196\n"
    "s_lshl_b32 s56, s65, 7\n"
    "s_waitcnt lgkmcnt(0)\n"
    "buffer_load_dword v242, v243, s[8:11], s56 offen\n"
    ".Lfb_tw2:\n"
    "s_cmp_lg_u32 s40, 1\n"
    "s_cbranch_scc1 .Lfb_tw3\n"
    "v_lshlrev_b32 v243, 15, v196\n"
    "s_lshl_b32 s56, s65, 7\n"
    "s_waitcnt vmcnt(0) lgkmcnt(0)\n"
    "buffer_load_dword v242, v243, s[4:7], s56 offen\n"
    ".Lfb_tw3:\n"
    "s_lshr_b32 s56, s65, 5\n"
    "s_lshl_b32 s56, s56, 2\n"
    "s_and_b32 s57, s65, 1\n"
    "s_mov_b32 s58, 0x10753642\n"
    "s_cselect_b32 s57, 0xfeca8db9, s58\n"
    "s_lshr_b32 s57, s57, s56\n"
    "s_and_b32 s42, s57, 15\n"
    "s_and_b32 s61, s40, 3\n"
    "s_mul_i32 s55, s61, 43008\n"
    "s_mul_i32 s57, s42, 56\n"
    "s_and_b32 s57, s57, 0xfff0\n"
    "s_add_u32 s73, s55, s57\n"
    "s_lshl_b32 s74, s61, 10\n"
    "v_and_b32 v200, 3, v196\n"
    "v_lshrrev_b32 v2, 2, v196\n"
    "v_mul_u32_u24 v210, 0xa80, v2\n"
    "v_lshl_add_u32 v210, v200, 4, v210\n"
    "s_bfe_u32 s56, s65, 0x20001\n"
    "s_bfe_u32 s57, s65, 0x20003\n"
    "s_lshl2_add_u32 s41, s56, s57\n"
    "s_lshr_b32 s56, s65, 6\n"
    "s_and_b32 s57, s65, 7\n"
    "s_lshl2_add_u32 s43, s57, s56\n"
    "s_bfe_u32 s44, s65, 0x30003\n"
    "s_xor_b32 s54, s42, 1\n"
    "s_lshr_b32 s62, s40, 2\n"
    "s_mul_i32 s57, s54, 56\n"
    "s_and_b32 s57, s57, 0xfff0\n"
    "s_add_u32 s55, s55, s57\n"
    "s_lshl2_add_u32 s56, s41, s61\n"
    "s_mul_i32 s75, s56, 28672\n"
    "s_lshl_b32 s45, s61, 12\n"
    "s_add_u32 s45, s45, 0x18000\n"
    "v_lshlrev_b32 v1, 4, v196\n"
    "v_lshrrev_b32 v198, 4, v196\n"
    "v_lshrrev_b32 v2, 3, v196\n"
    "v_mul_u32_u24 v211, 0x380, v2\n"
    "v_and_b32 v3, 7, v196\n"
    "v_xor_b32 v3, v3, v198\n"
    "v_lshl_add_u32 v211, v3, 4, v211\n"
    "v_xor_b32 v212, 64, v211\n"
    "v_add_u32 v212, 0x1800, v212\n"
    "s_cmp_gt_u32 s40, 3\n"
    "s_cbranch_scc1 .Lfb_rv4\n"
    "v_lshrrev_b32 v201, 5, v196\n"
    "v_and_b32 v197, 15, v196\n"
    "v_and_b32 v202, 31, v196\n"
    "v_bfe_u32 v199, v196, 2, 2\n"
    "v_bfe_u32 v2, v196, 1, 3\n"
    "v_xor_b32 v2, v2, v201\n"
    "v_lshlrev_b32 v2, 4, v2\n"
    "v_lshl_add_u32 v2, v202, 7, v2\n"
    "v_add_u32 v204, s45, v2\n"
    "v_xor_b32 v205, 0x20, v204\n"
    "v_xor_b32 v206, 0x40, v204\n"
    "v_xor_b32 v207, 0x60, v204\n"
    "v_and_b32 v4, 1, v198\n"
    "v_lshl_add_u32 v4, v4, 2, v200\n"
    "v_lshl_add_u32 v3, v201, 3, v199\n"
    "s_and_b32 s56, s42, 1\n"
    "v_add_lshl_u32 v2, v4, s56, 3\n"
    "v_and_b32 v2, 63, v2\n"
    "v_lshl_add_u32 v208, v3, 6, v2\n"
    "s_and_b32 s56, s54, 1\n"
    "v_add_lshl_u32 v2, v4, s56, 3\n"
    "v_and_b32 v2, 63, v2\n"
    "v_lshl_add_u32 v209, v3, 6, v2\n"
    "v_add_u32 v209, 0xe000, v209\n"
    "v_mov_b32 v228, 0x3f803f80\n"
    "v_mov_b32 v229, 0x3f803f80\n"
    "v_mov_b32 v230, 0x3f803f80\n"
    "v_mov_b32 v231, 0x3f803f80\n"
    "v_cmp_gt_u32 s[50:51], 32, v196\n"
    "v_cmp_le_u32 s[52:53], 32, v196\n"
    "s_lshl_b32 s56, s61, 5\n"
    "v_add_u32 v2, s56, v202\n"
    "v_mul_u32_u24 v213, 0xc8, v2\n"
    "v_lshl_add_u32 v213, v201, 3, v213\n"
    "v_add_u32 v213, 0x18000, v213\n"
    "v_lshlrev_b32 v220, 4, v2\n"
    "v_add_u32 v220, 0x16200, v220\n"
    "v_lshlrev_b32 v215, 2, v202\n"
    "v_add_u32 v215, 0x16000, v215\n"
    "v_mov_b32 v234, 0\n"
    "v_mov_b32 v235, 0\n"
    "s_lshr_b32 s57, s42, 1\n"
    "s_mul_i32 s57, s57, 3136\n"
    "s_add_u32 s57, s57, s74\n"
    "s_add_u32 m0, s74, 0x15000\n"
    "s_mov_b32 s56, s57\n"
    "s_waitcnt lgkmcnt(0)\n"
    "buffer_load_dwordx4 v1, s[16:19], s56 offen lds\n"
    "s_cmp_lg_u32 s40, 2\n"
    "s_cbranch_scc1 .Lfb_ft6\n"
    "s_mul_i32 s56, s43, 73728\n"
    "s_lshl_b32 s57, s44, 11\n"
    "s_add_u32 s56, s56, s57\n"
    "s_add_u32 s56, s56, 0x31df00\n"
    "s_lshl_b32 s57, s65, 8\n"
    "v_mov_b32 v2, s56\n"
    "v_mov_b32 v3, s57\n"
    "v_cmp_gt_u32 vcc, 2, v196\n"
    "s_nop 1\n"
    "v_cndmask_b32 v2, v2, v3, vcc\n"
    "v_lshl_add_u32 v2, v196, 7, v2\n"
    "s_mov_b64 exec, 0x3ffff\n"
    "s_waitcnt vmcnt(1)\n"
    "buffer_load_dword v242, v2, s[12:15], 0 offen sc1\n"
    "s_mov_b64 exec, -1\n"
    ".Lfb_ft6:\n"
    "buffer_load_dwordx2 v[218:219], off, s[12:15], 0 sc1\n"
    "s_mul_i32 s56, s61, 7168\n"
    "s_and_b32 s57, s42, 1\n"
    "s_cselect_b32 s57, 0, 56\n"
    "s_add_u32 s56, s56, s57\n"
    "v_lshlrev_b32 v5, 6, v196\n"
    "v_add_u32 v5, s56, v5\n"
    "v_bfe_u32 v2, v196, 3, 2\n"
    "v_lshl_add_u32 v2, v2, 2, v200\n"
    "v_cmp_lt_u32 s[48:49], 13, v2\n"
    "v_bfe_u32 v3, v196, 2, 1\n"
    "v_cmp_eq_u32 vcc, v3, v201\n"
    "v_mul_u32_u24 v3, 14, v3\n"
    "s_nop 0\n"
    "v_cndmask_b32 v4, 63, v2, vcc\n"
    "v_add_u32 v2, v2, v3\n"
    "v_mul_u32_u24 v3, 0x70, v201\n"
    "v_add_lshl_u32 v214, v2, v3, 1\n"
    "v_add_u32 v214, 0x15000, v214\n"
    "v_cmp_eq_u32 vcc, 0, v4\n"
    "s_nop 1\n"
    "v_cndmask_b32 v88, 0, 1.0, vcc\n"
    "v_cmp_eq_u32 vcc, 1, v4\n"
    "s_nop 1\n"
    "v_cndmask_b32 v89, 0, 1.0, vcc\n"
    "v_cmp_eq_u32 vcc, 2, v4\n"
    "s_nop 1\n"
    "v_cndmask_b32 v90, 0, 1.0, vcc\n"
    "v_cmp_eq_u32 vcc, 3, v4\n"
    "s_nop 1\n"
    "v_cndmask_b32 v91, 0, 1.0, vcc\n"
    "v_cmp_eq_u32 vcc, 4, v4\n"
    "s_nop 1\n"
    "v_cndmask_b32 v92, 0, 1.0, vcc\n"
    "v_cmp_eq_u32 vcc, 5, v4\n"
    "s_nop 1\n"
    "v_cndmask_b32 v93, 0, 1.0, vcc\n"
    "v_cmp_eq_u32 vcc, 6, v4\n"
    "s_nop 1\n"
    "v_cndmask_b32 v94, 0, 1.0, vcc\n"
    "v_cmp_eq_u32 vcc, 7, v4\n"
    "s_nop 1\n"
    "v_cndmask_b32 v95, 0, 1.0, vcc\n"
    "v_cmp_eq_u32 vcc, 8, v4\n"
    "s_nop 1\n"
    "v_cndmask_b32 v96, 0, 1.0, vcc\n"
    "v_cmp_eq_u32 vcc, 9, v4\n"
    "s_nop 1\n"
    "v_cndmask_b32 v97, 0, 1.0, vcc\n"
    "v_cmp_eq_u32 vcc, 10, v4\n"
    "s_nop 1\n"
    "v_cndmask_b32 v98, 0, 1.0, vcc\n"
    "v_cmp_eq_u32 vcc, 11, v4\n"
    "s_nop 1\n"
    "v_cndmask_b32 v99, 0, 1.0, vcc\n"
    "v_cmp_eq_u32 vcc, 12, v4\n"
    "s_nop 1\n"
    "v_cndmask_b32 v100, 0, 1.0, vcc\n"
    "v_cmp_eq_u32 vcc, 13, v4\n"
    "s_nop 1\n"
    "v_cndmask_b32 v101, 0, 1.0, vcc\n"
    "v_cmp_eq_u32 vcc, 14, v4\n"
    "s_nop 1\n"
    "v_cndmask_b32 v102, 0, 1.0, vcc\n"
    "v_cmp_eq_u32 vcc, 15, v4\n"
    "s_nop 1\n"
    "v_cndmask_b32 v103, 0, 1.0, vcc\n"
    "s_waitcnt vmcnt(0)\n"
    "v_readfirstlane_b32 s46, v218\n"
    "v_readfirstlane_b32 s47, v219\n"
    "s_add_u32 s46, s46, 1\n"
    "s_addc_u32 s47, s47, 0\n"
    "v_mov_b32 v6, s46\n"
    "v_mov_b32 v7, s47\n"
    "s_barrier\n"
    "s_and_b32 s56, s54, 1\n"
    "s_mul_i32 s56, s56, 1568\n"
    "v_add_u32 v2, s56, v214\n"
    "ds_read_u16 v104, v2\n"
    "ds_read_u16 v105, v2 offset:56\n"
    "ds_read_u16 v106, v2 offset:112\n"
    "ds_read_u16 v107, v2 offset:168\n"
    "ds_read_u16 v108, v2 offset:448\n"
    "ds_read_u16 v109, v2 offset:504\n"
    "ds_read_u16 v110, v2 offset:560\n"
    "ds_read_u16 v111, v2 offset:616\n"
    "ds_read_u16 v112, v2 offset:896\n"
    "ds_read_u16 v113, v2 offset:952\n"
    "ds_read_u16 v114, v2 offset:1008\n"
    "ds_read_u16 v115, v2 offset:1064\n"
    "ds_read_u16 v116, v2 offset:1344\n"
    "ds_read_u16 v117, v2 offset:1400\n"
    "ds_read_u16 v118, v2 offset:1456\n"
    "ds_read_u16 v119, v2 offset:1512\n"
    "s_waitcnt lgkmcnt(14)\n"
    "v_lshl_or_b32 v120, v105, 16, v104\n"
    "s_waitcnt lgkmcnt(12)\n"
    "v_lshl_or_b32 v121, v107, 16, v106\n"
    "s_waitcnt lgkmcnt(10)\n"
    "v_lshl_or_b32 v122, v109, 16, v108\n"
    "s_waitcnt lgkmcnt(8)\n"
    "v_lshl_or_b32 v123, v111, 16, v110\n"
    "s_waitcnt lgkmcnt(6)\n"
    "v_lshl_or_b32 v124, v113, 16, v112\n"
    "s_waitcnt lgkmcnt(4)\n"
    "v_lshl_or_b32 v125, v115, 16, v114\n"
    "s_waitcnt lgkmcnt(2)\n"
    "v_lshl_or_b32 v126, v117, 16, v116\n"
    "s_waitcnt lgkmcnt(0)\n"
    "v_lshl_or_b32 v127, v119, 16, v118\n"
    "v_cndmask_b32 v126, v126, 0, s[52:53]\n"
    "v_cndmask_b32 v127, v127, 0, s[52:53]\n"
    "v_cndmask_b32 v120, v120, 0, s[48:49]\n"
    "v_cndmask_b32 v121, v121, 0, s[48:49]\n"
    "v_cndmask_b32 v122, v122, 0, s[48:49]\n"
    "v_cndmask_b32 v123, v123, 0, s[48:49]\n"
    "v_cndmask_b32 v124, v124, 0, s[48:49]\n"
    "v_cndmask_b32 v125, v125, 0, s[48:49]\n"
    "v_cndmask_b32 v126, v126, 0, s[48:49]\n"
    "v_cndmask_b32 v127, v127, 0, s[48:49]\n"
    "s_and_b32 s56, s42, 1\n"
    "s_mul_i32 s56, s56, 1568\n"
    "v_add_u32 v2, s56, v214\n"
    "ds_read_u16 v104, v2\n"
    "ds_read_u16 v105, v2 offset:56\n"
    "ds_read_u16 v106, v2 offset:112\n"
    "ds_read_u16 v107, v2 offset:168\n"
    "ds_read_u16 v108, v2 offset:448\n"
    "ds_read_u16 v109, v2 offset:504\n"
    "ds_read_u16 v110, v2 offset:560\n"
    "ds_read_u16 v111, v2 offset:616\n"
    "ds_read_u16 v112, v2 offset:896\n"
    "ds_read_u16 v113, v2 offset:952\n"
    "ds_read_u16 v114, v2 offset:1008\n"
    "ds_read_u16 v115, v2 offset:1064\n"
    "ds_read_u16 v116, v2 offset:1344\n"
    "ds_read_u16 v117, v2 offset:1400\n"
    "ds_read_u16 v118, v2 offset:1456\n"
    "ds_read_u16 v119, v2 offset:1512\n"
    "v_add_u32 v3, 0x16a00, v1\n"
    "ds_write_b128 v3, v[120:123]\n"
    "ds_write_b128 v3, v[124:127] offset:1024\n"
    "s_waitcnt lgkmcnt(14)\n"
    "v_lshl_or_b32 v184, v105, 16, v104\n"
    "v_lshl_or_b32 v185, v107, 16, v106\n"
    "s_waitcnt lgkmcnt(12)\n"
    "v_lshl_or_b32 v186, v109, 16, v108\n"
    "s_waitcnt lgkmcnt(10)\n"
    "v_lshl_or_b32 v187, v111, 16, v110\n"
    "s_waitcnt lgkmcnt(8)\n"
    "v_lshl_or_b32 v188, v113, 16, v112\n"
    "s_waitcnt lgkmcnt(6)\n"
    "v_lshl_or_b32 v189, v115, 16, v114\n"
    "s_waitcnt lgkmcnt(4)\n"
    "v_lshl_or_b32 v190, v117, 16, v116\n"
    "s_waitcnt lgkmcnt(2)\n"
    "v_lshl_or_b32 v191, v119, 16, v118\n"
    "v_cndmask_b32 v190, v190, 0, s[52:53]\n"
    "v_cndmask_b32 v191, v191, 0, s[52:53]\n"
    "v_cndmask_b32 v184, v184, 0, s[48:49]\n"
    "v_cndmask_b32 v185, v185, 0, s[48:49]\n"
    "v_cndmask_b32 v186, v186, 0, s[48:49]\n"
    "v_cndmask_b32 v187, v187, 0, s[48:49]\n"
    "v_cndmask_b32 v188, v188, 0, s[48:49]\n"
    "v_cndmask_b32 v189, v189, 0, s[48:49]\n"
    "v_cndmask_b32 v190, v190, 0, s[48:49]\n"
    "v_cndmask_b32 v191, v191, 0, s[48:49]\n"
    "s_barrier\n"
    "v_mov_b32 v2, 0x17200\n"
    "ds_write_b64 v2, v[234:235]\n"
    "ds_write_b16 v5, v228\n"
    "s_mov_b32 exec_hi, 0xffff\n"
    "ds_write_b16 v5, v228 offset:4096\n"
    "s_mov_b32 exec_hi, -1\n"
    "s_waitcnt lgkmcnt(0)\n"
    "s_barrier\n"
    "ds_read_b128 v[8:11], v204\n"
    "ds_read_b128 v[12:15], v205\n"
    "ds_read_b128 v[16:19], v206\n"
    "ds_read_b128 v[20:23], v207\n"
    "s_add_u32 m0, s45, 0xc000\n"
    "s_add_u32 s56, s75, 0x3180\n"
    "buffer_load_dwordx4 v211, s[4:7], s56 offen offset:2048 lds\n"
    "buffer_load_dwordx4 v212, s[4:7], s56 offen offset:3072 lds\n"
    "ds_read_b64_tr_b16 v[40:41], v208\n"
    "ds_read_b64_tr_b16 v[42:43], v208 offset:256\n"
    "ds_read_b64_tr_b16 v[44:45], v208 offset:1024\n"
    "ds_read_b64_tr_b16 v[46:47], v208 offset:1280\n"
    "s_waitcnt lgkmcnt(2)\n"
    "v_mfma_f32_32x32x16_bf16 v[56:71], v[40:43], v[8:11], 0\n"
    "s_lshl_b32 s56, s61, 10\n"
    "v_add_u32 v221, s56, v208\n"
    "ds_read_b64_tr_b16 v[104:105], v221\n"
    "ds_read_b64_tr_b16 v[48:49], v208 offset:2048\n"
    "ds_read_b64_tr_b16 v[106:107], v221 offset:256\n"
    "ds_read_b64_tr_b16 v[108:109], v221 offset:4096\n"
    "ds_read_b64_tr_b16 v[110:111], v221 offset:4352\n"
    "v_mfma_f32_32x32x16_bf16 v[72:87], v[8:11], v[8:11], 0\n"
    "ds_read_b64_tr_b16 v[112:113], v221 offset:8192\n"
    "ds_read_b64_tr_b16 v[114:115], v221 offset:8448\n"
    "ds_read_b64_tr_b16 v[50:51], v208 offset:2304\n"
    "ds_read_b64_tr_b16 v[116:117], v221 offset:12288\n"
    "ds_read_b64_tr_b16 v[118:119], v221 offset:12544\n"
    "s_waitcnt lgkmcnt(10)\n"
    "v_mfma_f32_32x32x16_bf16 v[56:71], v[44:47], v[12:15], v[56:71]\n"
    "ds_read_b64_tr_b16 v[120:121], v221 offset:16384\n"
    "ds_read_b64_tr_b16 v[122:123], v221 offset:16640\n"
    "ds_read_b64_tr_b16 v[52:53], v208 offset:3072\n"
    "ds_read_b64_tr_b16 v[124:125], v221 offset:20480\n"
    "ds_read_b64_tr_b16 v[126:127], v221 offset:20736\n"
    "v_mfma_f32_32x32x16_bf16 v[72:87], v[12:15], v[12:15], v[72:87]\n"
    "ds_read_b64_tr_b16 v[128:129], v221 offset:24576\n"
    "ds_read_b64_tr_b16 v[130:131], v221 offset:24832\n"
    "ds_read_b64_tr_b16 v[54:55], v208 offset:3328\n"
    "s_lshl_b32 s56, s61, 7\n"
    "v_add_u32 v221, s56, v215\n"
    "s_barrier\n"
    "ds_read_b128 v[24:27], v204 offset:16384\n"
    "ds_read_b128 v[28:31], v205 offset:16384\n"
    "ds_read_b128 v[32:35], v206 offset:16384\n"
    "ds_read_b128 v[36:39], v207 offset:16384\n"
    "s_mov_b32 m0, s45\n"
    "s_add_u32 s56, s75, 0x3200\n"
    "buffer_load_dwordx4 v211, s[4:7], s56 offen offset:2048 lds\n"
    "buffer_load_dwordx4 v212, s[4:7], s56 offen offset:3072 lds\n"
    "s_waitcnt lgkmcnt(14)\n"
    "v_mfma_f32_32x32x16_bf16 v[56:71], v[48:51], v[16:19], v[56:71]\n"
    "v_mfma_f32_32x32x16_bf16 v[132:147], v[228:231], v[104:107], 0\n"
    "ds_read_b64_tr_b16 v[40:41], v208 offset:4096\n"
    "v_mfma_f32_32x32x16_bf16 v[132:147], v[228:231], v[108:111], v[132:147]\n"
    "v_mfma_f32_32x32x16_bf16 v[72:87], v[16:19], v[16:19], v[72:87]\n"
    "v_mfma_f32_32x32x16_bf16 v[132:147], v[228:231], v[112:115], v[132:147]\n"
    "ds_read_b64_tr_b16 v[42:43], v208 offset:4352\n"
    "s_waitcnt lgkmcnt(14)\n"
    "v_mfma_f32_32x32x16_bf16 v[132:147], v[228:231], v[116:119], v[132:147]\n"
    "s_waitcnt lgkmcnt(6)\n"
    "v_mfma_f32_32x32x16_bf16 v[56:71], v[52:55], v[20:23], v[56:71]\n"
    "ds_read_b64_tr_b16 v[44:45], v208 offset:5120\n"
    "v_mfma_f32_32x32x16_bf16 v[72:87], v[20:23], v[20:23], v[72:87]\n"
    "ds_read_b64_tr_b16 v[46:47], v208 offset:5376\n"
    "s_waitcnt lgkmcnt(2)\n"
    "v_mfma_f32_32x32x16_bf16 v[56:71], v[40:43], v[24:27], v[56:71]\n"
    "ds_read_b64_tr_b16 v[48:49], v208 offset:6144\n"
    "v_mfma_f32_32x32x16_bf16 v[72:87], v[24:27], v[24:27], v[72:87]\n"
    "ds_read_b64_tr_b16 v[50:51], v208 offset:6400\n"
    "s_waitcnt lgkmcnt(2)\n"
    "v_mfma_f32_32x32x16_bf16 v[56:71], v[44:47], v[28:31], v[56:71]\n"
    "ds_read_b64_tr_b16 v[52:53], v208 offset:7168\n"
    "v_mfma_f32_32x32x16_bf16 v[72:87], v[28:31], v[28:31], v[72:87]\n"
    "ds_read_b64_tr_b16 v[54:55], v208 offset:7424\n"
    "s_barrier\n"
    "ds_read_b128 v[8:11], v204 offset:32768\n"
    "ds_read_b128 v[12:15], v205 offset:32768\n"
    "ds_read_b128 v[16:19], v206 offset:32768\n"
    "ds_read_b128 v[20:23], v207 offset:32768\n"
    "s_add_u32 m0, s45, 0x4000\n"
    "s_add_u32 s56, s75, 0x3280\n"
    "buffer_load_dwordx4 v211, s[4:7], s56 offen offset:2048 lds\n"
    "buffer_load_dwordx4 v212, s[4:7], s56 offen offset:3072 lds\n"
    "s_waitcnt lgkmcnt(6)\n"
    "v_mfma_f32_32x32x16_bf16 v[56:71], v[48:51], v[32:35], v[56:71]\n"
    "v_mfma_f32_32x32x16_bf16 v[132:147], v[228:231], v[120:123], v[132:147]\n"
    "ds_read_b64_tr_b16 v[40:41], v208 offset:8192\n"
    "v_mfma_f32_32x32x16_bf16 v[132:147], v[228:231], v[124:127], v[132:147]\n"
    "v_mfma_f32_32x32x16_bf16 v[72:87], v[32:35], v[32:35], v[72:87]\n"
    "v_mfma_f32_32x32x16_bf16 v[132:147], v[228:231], v[128:131], v[132:147]\n"
    "ds_read_b64_tr_b16 v[42:43], v208 offset:8448\n"
    "s_waitcnt lgkmcnt(6)\n"
    "v_mfma_f32_32x32x16_bf16 v[56:71], v[52:55], v[36:39], v[56:71]\n"
    "ds_read_b64_tr_b16 v[44:45], v208 offset:9216\n"
    "v_mfma_f32_32x32x16_bf16 v[72:87], v[36:39], v[36:39], v[72:87]\n"
    "ds_read_b64_tr_b16 v[46:47], v208 offset:9472\n"
    "s_waitcnt lgkmcnt(2)\n"
    "v_mfma_f32_32x32x16_bf16 v[56:71], v[40:43], v[8:11], v[56:71]\n"
    "ds_read_b64_tr_b16 v[48:49], v208 offset:10240\n"
    "v_mfma_f32_32x32x16_bf16 v[72:87], v[8:11], v[8:11], v[72:87]\n"
    "ds_read_b64_tr_b16 v[50:51], v208 offset:10496\n"
    "s_waitcnt lgkmcnt(2)\n"
    "v_mfma_f32_32x32x16_bf16 v[56:71], v[44:47], v[12:15], v[56:71]\n"
    "ds_read_b64_tr_b16 v[52:53], v208 offset:11264\n"
    "v_mfma_f32_32x32x16_bf16 v[72:87], v[12:15], v[12:15], v[72:87]\n"
    "ds_read_b64_tr_b16 v[54:55], v208 offset:11520\n"
    "s_waitcnt vmcnt(4)\n"
    "s_barrier\n"
    "ds_read_b128 v[24:27], v204 offset:49152\n"
    "ds_read_b128 v[28:31], v205 offset:49152\n"
    "ds_read_b128 v[32:35], v206 offset:49152\n"
    "ds_read_b128 v[36:39], v207 offset:49152\n"
    "s_add_u32 m0, s45, 0x8000\n"
    "s_add_u32 s56, s75, 0x3300\n"
    "buffer_load_dwordx4 v211, s[4:7], s56 offen offset:2048 lds\n"
    "buffer_load_dwordx4 v212, s[4:7], s56 offen offset:3072 lds\n"
    "s_waitcnt lgkmcnt(6)\n"
    "v_mfma_f32_32x32x16_bf16 v[56:71], v[48:51], v[16:19], v[56:71]\n"
    "ds_write_b32 v221, v132\n"
    "ds_read_b64_tr_b16 v[40:41], v208 offset:12288\n"
    "v_mfma_f32_32x32x16_bf16 v[72:87], v[16:19], v[16:19], v[72:87]\n"
    "ds_read_b64_tr_b16 v[42:43], v208 offset:12544\n"
    "s_waitcnt lgkmcnt(7)\n"
    "v_mfma_f32_32x32x16_bf16 v[56:71], v[52:55], v[20:23], v[56:71]\n"
    "ds_read_b64_tr_b16 v[44:45], v208 offset:13312\n"
    "v_mfma_f32_32x32x16_bf16 v[72:87], v[20:23], v[20:23], v[72:87]\n"
    "ds_read_b64_tr_b16 v[46:47], v208 offset:13568\n"
    "s_waitcnt lgkmcnt(2)\n"
    "v_mfma_f32_32x32x16_bf16 v[56:71], v[40:43], v[24:27], v[56:71]\n"
    "ds_read_b64_tr_b16 v[48:49], v208 offset:14336\n"
    "v_mfma_f32_32x32x16_bf16 v[72:87], v[24:27], v[24:27], v[72:87]\n"
    "ds_read_b64_tr_b16 v[50:51], v208 offset:14592\n"
    "s_waitcnt lgkmcnt(2)\n"
    "v_mfma_f32_32x32x16_bf16 v[56:71], v[44:47], v[28:31], v[56:71]\n"
    "ds_read_b64_tr_b16 v[52:53], v208 offset:15360\n"
    "v_mfma_f32_32x32x16_bf16 v[72:87], v[28:31], v[28:31], v[72:87]\n"
    "ds_read_b64_tr_b16 v[54:55], v208 offset:15616\n"
    "s_waitcnt vmcnt(4)\n"
    "s_barrier\n"
    "ds_read_b128 v[8:11], v204\n"
    "ds_read_b128 v[12:15], v205\n"
    "ds_read_b128 v[16:19], v206\n"
    "ds_read_b128 v[20:23], v207\n"
    "s_waitcnt lgkmcnt(6)\n"
    "v_mfma_f32_32x32x16_bf16 v[56:71], v[48:51], v[32:35], v[56:71]\n"
    "ds_read_b32 v128, v215\n"
    "ds_read_b64_tr_b16 v[40:41], v208 offset:16384\n"
    "ds_read_b32 v129, v215 offset:128\n"
    "v_mfma_f32_32x32x16_bf16 v[72:87], v[32:35], v[32:35], v[72:87]\n"
    "ds_read_b32 v130, v215 offset:256\n"
    "ds_read_b64_tr_b16 v[42:43], v208 offset:16640\n"
    "ds_read_b32 v131, v215 offset:384\n"
    "s_waitcnt lgkmcnt(10)\n"
    "v_mfma_f32_32x32x16_bf16 v[56:71], v[52:55], v[36:39], v[56:71]\n"
    "ds_read_b64_tr_b16 v[44:45], v208 offset:17408\n"
    "v_mfma_f32_32x32x16_bf16 v[72:87], v[36:39], v[36:39], v[72:87]\n"
    "ds_read_b64_tr_b16 v[46:47], v208 offset:17664\n"
    "s_waitcnt lgkmcnt(3)\n"
    "v_mfma_f32_32x32x16_bf16 v[56:71], v[40:43], v[8:11], v[56:71]\n"
    "ds_read_b64_tr_b16 v[48:49], v208 offset:18432\n"
    "v_mfma_f32_32x32x16_bf16 v[72:87], v[8:11], v[8:11], v[72:87]\n"
    "ds_read_b64_tr_b16 v[50:51], v208 offset:18688\n"
    "s_waitcnt lgkmcnt(2)\n"
    "v_mfma_f32_32x32x16_bf16 v[56:71], v[44:47], v[12:15], v[56:71]\n"
    "ds_read_b64_tr_b16 v[52:53], v208 offset:19456\n"
    "v_mfma_f32_32x32x16_bf16 v[72:87], v[12:15], v[12:15], v[72:87]\n"
    "ds_read_b64_tr_b16 v[54:55], v208 offset:19712\n"
    "s_waitcnt vmcnt(2)\n"
    "s_barrier\n"
    "ds_read_b128 v[24:27], v204 offset:16384\n"
    "ds_read_b128 v[28:31], v205 offset:16384\n"
    "ds_read_b128 v[32:35], v206 offset:16384\n"
    "ds_read_b128 v[36:39], v207 offset:16384\n"
    "s_waitcnt lgkmcnt(6)\n"
    "v_mfma_f32_32x32x16_bf16 v[56:71], v[48:51], v[16:19], v[56:71]\n"
    "v_add_f32 v128, v128, v129\n"
    "ds_read_b64_tr_b16 v[40:41], v208 offset:20480\n"
    "v_add_f32 v130, v130, v131\n"
    "v_mfma_f32_32x32x16_bf16 v[72:87], v[16:19], v[16:19], v[72:87]\n"
    "v_sub_f32 v128, -v128, v130\n"
    "ds_read_b64_tr_b16 v[42:43], v208 offset:20736\n"
    "v_cvt_pk_bf16_f32 v232, v128, v128\n"
    "s_waitcnt lgkmcnt(6)\n"
    "v_mfma_f32_32x32x16_bf16 v[56:71], v[52:55], v[20:23], v[56:71]\n"
    "v_lshlrev_b32 v129, 16, v232\n"
    "ds_read_b64_tr_b16 v[44:45], v208 offset:21504\n"
    "v_sub_f32 v128, v128, v129\n"
    "v_mfma_f32_32x32x16_bf16 v[72:87], v[20:23], v[20:23], v[72:87]\n"
    "v_cvt_pk_bf16_f32 v233, v128, 0\n"
    "ds_read_b64_tr_b16 v[46:47], v208 offset:21760\n"
    "v_cndmask_b32 v232, 0, v232, s[50:51]\n"
    "s_waitcnt lgkmcnt(2)\n"
    "v_mfma_f32_32x32x16_bf16 v[56:71], v[40:43], v[24:27], v[56:71]\n"
    "v_cndmask_b32 v233, 0, v233, s[50:51]\n"
    "ds_read_b64_tr_b16 v[48:49], v208 offset:22528\n"
    "v_mfma_f32_32x32x16_bf16 v[72:87], v[24:27], v[24:27], v[72:87]\n"
    "ds_read_b64_tr_b16 v[50:51], v208 offset:22784\n"
    "s_waitcnt lgkmcnt(2)\n"
    "v_mfma_f32_32x32x16_bf16 v[56:71], v[44:47], v[28:31], v[56:71]\n"
    "ds_read_b64_tr_b16 v[52:53], v208 offset:23552\n"
    "v_mfma_f32_32x32x16_bf16 v[72:87], v[28:31], v[28:31], v[72:87]\n"
    "ds_read_b64_tr_b16 v[54:55], v208 offset:23808\n"
    "s_waitcnt vmcnt(0)\n"
    "s_barrier\n"
    "ds_read_b128 v[8:11], v204 offset:32768\n"
    "ds_read_b128 v[12:15], v205 offset:32768\n"
    "ds_read_b128 v[16:19], v206 offset:32768\n"
    "ds_read_b128 v[20:23], v207 offset:32768\n"
    "s_waitcnt lgkmcnt(6)\n"
    "v_mfma_f32_32x32x16_bf16 v[56:71], v[48:51], v[32:35], v[56:71]\n"
    "ds_read_b64_tr_b16 v[40:41], v208 offset:24576\n"
    "v_mfma_f32_32x32x16_bf16 v[72:87], v[32:35], v[32:35], v[72:87]\n"
    "ds_read_b64_tr_b16 v[42:43], v208 offset:24832\n"
    "s_waitcnt lgkmcnt(6)\n"
    "v_mfma_f32_32x32x16_bf16 v[56:71], v[52:55], v[36:39], v[56:71]\n"
    "ds_read_b64_tr_b16 v[44:45], v208 offset:25600\n"
    "v_mfma_f32_32x32x16_bf16 v[72:87], v[36:39], v[36:39], v[72:87]\n"
    "ds_read_b64_tr_b16 v[46:47], v208 offset:25856\n"
    "s_waitcnt lgkmcnt(2)\n"
    "v_mfma_f32_32x32x16_bf16 v[56:71], v[40:43], v[8:11], v[56:71]\n"
    "ds_read_b64_tr_b16 v[48:49], v208 offset:26624\n"
    "v_mfma_f32_32x32x16_bf16 v[72:87], v[8:11], v[8:11], v[72:87]\n"
    "ds_read_b64_tr_b16 v[50:51], v208 offset:26880\n"
    "s_waitcnt lgkmcnt(2)\n"
    "v_mfma_f32_32x32x16_bf16 v[56:71], v[44:47], v[12:15], v[56:71]\n"
    "ds_read_b64_tr_b16 v[52:53], v208 offset:27648\n"
    "v_mfma_f32_32x32x16_bf16 v[72:87], v[12:15], v[12:15], v[72:87]\n"
    "ds_read_b64_tr_b16 v[54:55], v208 offset:27904\n"
    "s_waitcnt lgkmcnt(2)\n"
    "v_mfma_f32_32x32x16_bf16 v[56:71], v[48:51], v[16:19], v[56:71]\n"
    "v_mfma_f32_32x32x16_bf16 v[72:87], v[16:19], v[16:19], v[72:87]\n"
    "s_waitcnt lgkmcnt(0)\n"
    "v_mfma_f32_32x32x16_bf16 v[56:71], v[52:55], v[20:23], v[56:71]\n"
    "v_mfma_f32_32x32x16_bf16 v[72:87], v[20:23], v[20:23], v[72:87]\n"
    "s_cmp_lg_u32 s40, 0\n"
    "s_cbranch_scc1 .Lfb_tw7\n"
    "v_lshlrev_b32 v243, 15, v196\n"
    "s_lshl_b32 s56, s65, 7\n"
    "buffer_load_dword v242, v243, s[20:23], s56 offen\n"
    ".Lfb_tw7:\n"
    "s_cmp_lg_u32 s40, 1\n"
    "s_cbranch_scc1 .Lfb_tw8\n"
    "v_lshlrev_b32 v243, 15, v196\n"
    "s_lshl_b32 s56, s65, 7\n"
    "s_waitcnt vmcnt(0)\n"
    "buffer_load_dword v242, v243, s[24:27], s56 offen\n"
    ".Lfb_tw8:\n"
    "s_cmp_lg_u32 s40, 2\n"
    "s_cbranch_scc1 .Lfb_tw9\n"
    "v_lshlrev_b32 v243, 15, v196\n"
    "s_lshl_b32 s56, s65, 7\n"
    "s_waitcnt vmcnt(0)\n"
    "buffer_load_dword v242, v243, s[28:31], s56 offen\n"
    ".Lfb_tw9:\n"
    "s_nop 4\n"
    "v_mov_b32 v2, v68\n"
    "v_mov_b32 v3, v68\n"
    "v_mov_b32 v238, 0\n"
    "s_nop 0\n"
    "v_permlane32_swap_b32 v2, v3\n"
    "v_mul_f32 v222, 0x3b124925, v3\n"
    "v_cvt_pk_bf16_f32 v237, v222, 0\n"
    "v_lshlrev_b32 v2, 16, v237\n"
    "v_sub_f32 v2, v222, v2\n"
    "v_cvt_pk_bf16_f32 v236, v222, v2\n"
    "v_mov_b32 v239, 0\n"
    "v_mul_f32 v240, v72, v88\n"
    "s_nop 0\n"
    "v_mfma_f32_32x32x16_bf16 v[56:71], v[232:235], v[236:239], v[56:71]\n"
    "v_fmac_f32 v240, v73, v89\n"
    "v_fmac_f32 v240, v74, v90\n"
    "v_fmac_f32 v240, v75, v91\n"
    "v_fmac_f32 v240, v76, v92\n"
    "v_fmac_f32 v240, v77, v93\n"
    "v_fmac_f32 v240, v78, v94\n"
    "v_fmac_f32 v240, v79, v95\n"
    "v_fmac_f32 v240, v80, v96\n"
    "v_fmac_f32 v240, v81, v97\n"
    "v_fmac_f32 v240, v82, v98\n"
    "v_fmac_f32 v240, v83, v99\n"
    "s_nop 0\n"
    "v_cvt_pk_bf16_f32 v104, v56, v57\n"
    "v_cvt_pk_bf16_f32 v105, v58, v59\n"
    "v_cvt_pk_bf16_f32 v106, v60, v61\n"
    "v_cvt_pk_bf16_f32 v107, v62, v63\n"
    "v_cvt_pk_bf16_f32 v108, v64, v65\n"
    "v_cvt_pk_bf16_f32 v109, v66, v67\n"
    "v_cvt_pk_bf16_f32 v110, v68, v69\n"
    "v_cvt_pk_bf16_f32 v111, v70, v71\n"
    "v_fmac_f32 v240, v84, v100\n"
    "v_fmac_f32 v240, v85, v101\n"
    "v_mfma_f32_32x32x16_bf16 v[112:127], v[184:187], v[104:107], 0\n"
    "v_mfma_f32_32x32x16_bf16 v[112:127], v[188:191], v[108:111], v[112:127]\n"
    "v_fmac_f32 v240, v86, v102\n"
    "v_fmac_f32 v240, v87, v103\n"
    "v_mov_b32 v241, v240\n"
    "v_mul_f32 v226, v222, v222\n"
    "s_nop 0\n"
    "v_permlane32_swap_b32 v240, v241\n"
    "v_add_f32 v240, v240, v241\n"
    "v_mul_f32 v240, 0x3b124925, v240\n"
    "v_sub_f32 v240, v240, v226\n"
    "v_max_f32 v240, 0, v240\n"
    "v_add_f32 v240, 0x3727c5ac, v240\n"
    "v_rsq_f32 v239, v240\n"
    "s_nop 0\n"
    "v_mul_f32 v224, 0x3fb8aa3b, v239\n"
    "v_max3_f32 v160, v112, v113, v114\n"
    "v_max3_f32 v161, -v112, -v113, -v114\n"
    "v_max3_f32 v160, v160, v115, v116\n"
    "v_max3_f32 v161, v161, -v115, -v116\n"
    "v_max3_f32 v160, v160, v117, v118\n"
    "v_max3_f32 v161, v161, -v117, -v118\n"
    "v_max3_f32 v160, v160, v119, v120\n"
    "v_max3_f32 v161, v161, -v119, -v120\n"
    "v_max3_f32 v160, v160, v121, v122\n"
    "v_max3_f32 v161, v161, -v121, -v122\n"
    "v_max3_f32 v160, v160, v123, v124\n"
    "v_max3_f32 v161, v161, -v123, -v124\n"
    "v_max_f32 v160, v160, v125\n"
    "v_max_f32 v161, v161, -v125\n"
    "ds_write_b128 v220, v[236:239]\n"
    "s_nop 0\n"
    "v_permlane32_swap_b32 v160, v161\n"
    "v_max_f32 v160, v160, v161\n"
    "v_mul_f32 v160, v224, v160\n"
    "v_mov_b32 v161, v160\n"
    "s_nop 1\n"
    "v_permlane32_swap_b32 v160, v161\n"
    "v_pk_fma_f32 v[128:129], v[112:113], v[224:225], v[160:161] op_sel_hi:[1,0,0] neg_lo:[0,0,1] neg_hi:[0,0,1]\n"
    "v_pk_fma_f32 v[144:145], v[112:113], v[224:225], v[160:161] op_sel:[0,0,1] op_sel_hi:[1,0,1] neg_lo:[1,0,1] neg_hi:[1,0,1]\n"
    "v_pk_fma_f32 v[130:131], v[114:115], v[224:225], v[160:161] op_sel_hi:[1,0,0] neg_lo:[0,0,1] neg_hi:[0,0,1]\n"
    "v_pk_fma_f32 v[146:147], v[114:115], v[224:225], v[160:161] op_sel:[0,0,1] op_sel_hi:[1,0,1] neg_lo:[1,0,1] neg_hi:[1,0,1]\n"
    "v_pk_fma_f32 v[132:133], v[116:117], v[224:225], v[160:161] op_sel_hi:[1,0,0] neg_lo:[0,0,1] neg_hi:[0,0,1]\n"
    "v_pk_fma_f32 v[148:149], v[116:117], v[224:225], v[160:161] op_sel:[0,0,1] op_sel_hi:[1,0,1] neg_lo:[1,0,1] neg_hi:[1,0,1]\n"
    "v_pk_fma_f32 v[134:135], v[118:119], v[224:225], v[160:161] op_sel_hi:[1,0,0] neg_lo:[0,0,1] neg_hi:[0,0,1]\n"
    "v_pk_fma_f32 v[150:151], v[118:119], v[224:225], v[160:161] op_sel:[0,0,1] op_sel_hi:[1,0,1] neg_lo:[1,0,1] neg_hi:[1,0,1]\n"
    "v_pk_fma_f32 v[136:137], v[120:121], v[224:225], v[160:161] op_sel_hi:[1,0,0] neg_lo:[0,0,1] neg_hi:[0,0,1]\n"
    "v_pk_fma_f32 v[152:153], v[120:121], v[224:225], v[160:161] op_sel:[0,0,1] op_sel_hi:[1,0,1] neg_lo:[1,0,1] neg_hi:[1,0,1]\n"
    "v_pk_fma_f32 v[138:139], v[122:123], v[224:225], v[160:161] op_sel_hi:[1,0,0] neg_lo:[0,0,1] neg_hi:[0,0,1]\n"
    "v_pk_fma_f32 v[154:155], v[122:123], v[224:225], v[160:161] op_sel:[0,0,1] op_sel_hi:[1,0,1] neg_lo:[1,0,1] neg_hi:[1,0,1]\n"
    "v_pk_fma_f32 v[140:141], v[124:125], v[224:225], v[160:161] op_sel_hi:[1,0,0] neg_lo:[0,0,1] neg_hi:[0,0,1]\n"
    "v_pk_fma_f32 v[156:157], v[124:125], v[224:225], v[160:161] op_sel:[0,0,1] op_sel_hi:[1,0,1] neg_lo:[1,0,1] neg_hi:[1,0,1]\n"
    "v_exp_f32 v128, v128\n"
    "v_exp_f32 v144, v144\n"
    "v_exp_f32 v129, v129\n"
    "v_exp_f32 v145, v145\n"
    "v_exp_f32 v130, v130\n"
    "v_exp_f32 v146, v146\n"
    "v_exp_f32 v131, v131\n"
    "v_exp_f32 v147, v147\n"
    "v_pk_add_f32 v[160:161], v[128:129], v[130:131]\n"
    "v_pk_add_f32 v[162:163], v[144:145], v[146:147]\n"
    "v_exp_f32 v132, v132\n"
    "v_exp_f32 v148, v148\n"
    "v_exp_f32 v133, v133\n"
    "v_exp_f32 v149, v149\n"
    "v_pk_add_f32 v[160:161], v[160:161], v[132:133]\n"
    "v_pk_add_f32 v[162:163], v[162:163], v[148:149]\n"
    "v_exp_f32 v134, v134\n"
    "v_exp_f32 v150, v150\n"
    "v_exp_f32 v135, v135\n"
    "v_exp_f32 v151, v151\n"
    "v_pk_add_f32 v[160:161], v[160:161], v[134:135]\n"
    "v_pk_add_f32 v[162:163], v[162:163], v[150:151]\n"
    "v_exp_f32 v136, v136\n"
    "v_exp_f32 v152, v152\n"
    "v_exp_f32 v137, v137\n"
    "v_exp_f32 v153, v153\n"
    "v_pk_add_f32 v[160:161], v[160:161], v[136:137]\n"
    "v_pk_add_f32 v[162:163], v[162:163], v[152:153]\n"
    "v_exp_f32 v138, v138\n"
    "v_exp_f32 v154, v154\n"
    "v_exp_f32 v139, v139\n"
    "v_exp_f32 v155, v155\n"
    "v_pk_add_f32 v[160:161], v[160:161], v[138:139]\n"
    "v_pk_add_f32 v[162:163], v[162:163], v[154:155]\n"
    "v_exp_f32 v140, v140\n"
    "v_exp_f32 v156, v156\n"
    "v_exp_f32 v141, v141\n"
    "v_exp_f32 v157, v157\n"
    "v_pk_add_f32 v[160:161], v[160:161], v[140:141]\n"
    "v_pk_add_f32 v[162:163], v[162:163], v[156:157]\n"
    "v_add_f32 v160, v160, v161\n"
    "v_add_f32 v161, v162, v163\n"
    "s_nop 1\n"
    "v_permlane32_swap_b32 v160, v161\n"
    "v_add_f32 v160, v160, v161\n"
    "v_rcp_f32 v160, v160\n"
    "s_nop 0\n"
    "v_mov_b32 v161, v160\n"
    "v_mov_b32 v175, 0\n"
    "v_mov_b32 v183, 0\n"
    "v_permlane32_swap_b32 v160, v161\n"
    "v_pk_mul_f32 v[128:129], v[128:129], v[160:161] op_sel_hi:[1,0]\n"
    "v_pk_mul_f32 v[144:145], v[144:145], v[160:161] op_sel:[0,1] op_sel_hi:[1,1]\n"
    "v_pk_mul_f32 v[130:131], v[130:131], v[160:161] op_sel_hi:[1,0]\n"
    "v_pk_mul_f32 v[146:147], v[146:147], v[160:161] op_sel:[0,1] op_sel_hi:[1,1]\n"
    "v_pk_mul_f32 v[132:133], v[132:133], v[160:161] op_sel_hi:[1,0]\n"
    "v_pk_mul_f32 v[148:149], v[148:149], v[160:161] op_sel:[0,1] op_sel_hi:[1,1]\n"
    "v_pk_mul_f32 v[134:135], v[134:135], v[160:161] op_sel_hi:[1,0]\n"
    "v_pk_mul_f32 v[150:151], v[150:151], v[160:161] op_sel:[0,1] op_sel_hi:[1,1]\n"
    "v_pk_mul_f32 v[136:137], v[136:137], v[160:161] op_sel_hi:[1,0]\n"
    "v_pk_mul_f32 v[152:153], v[152:153], v[160:161] op_sel:[0,1] op_sel_hi:[1,1]\n"
    "v_pk_mul_f32 v[138:139], v[138:139], v[160:161] op_sel_hi:[1,0]\n"
    "v_pk_mul_f32 v[154:155], v[154:155], v[160:161] op_sel:[0,1] op_sel_hi:[1,1]\n"
    "v_pk_mul_f32 v[140:141], v[140:141], v[160:161] op_sel_hi:[1,0]\n"
    "v_pk_mul_f32 v[156:157], v[156:157], v[160:161] op_sel:[0,1] op_sel_hi:[1,1]\n"
    "v_cvt_pk_bf16_f32 v168, v128, v129\n"
    "v_cvt_pk_bf16_f32 v176, v144, v145\n"
    "v_cvt_pk_bf16_f32 v169, v130, v131\n"
    "v_cvt_pk_bf16_f32 v177, v146, v147\n"
    "v_cvt_pk_bf16_f32 v170, v132, v133\n"
    "v_cvt_pk_bf16_f32 v178, v148, v149\n"
    "v_cvt_pk_bf16_f32 v171, v134, v135\n"
    "v_cvt_pk_bf16_f32 v179, v150, v151\n"
    "v_cvt_pk_bf16_f32 v172, v136, v137\n"
    "v_cvt_pk_bf16_f32 v180, v152, v153\n"
    "v_cvt_pk_bf16_f32 v173, v138, v139\n"
    "v_cvt_pk_bf16_f32 v181, v154, v155\n"
    "v_cvt_pk_bf16_f32 v174, v140, v141\n"
    "v_cvt_pk_bf16_f32 v182, v156, v157\n"
    "ds_write2_b64 v213, v[168:169], v[176:177] offset0:0 offset1:8\n"
    "ds_write2_b64 v213, v[170:171], v[178:179] offset0:2 offset1:10\n"
    "ds_write2_b64 v213, v[172:173], v[180:181] offset0:4 offset1:12\n"
    "ds_write2_b64 v213, v[174:175], v[182:183] offset0:6 offset1:14\n"
    "s_waitcnt lgkmcnt(0)\n"
    "s_barrier\n"
    "s_lshl_b32 s56, s61, 10\n"
    "v_add_u32 v221, s56, v208\n"
    "ds_read_b64_tr_b16 v[104:105], v221 offset:28672\n"
    "ds_read_b64_tr_b16 v[106:107], v221 offset:28928\n"
    "ds_read_b64_tr_b16 v[108:109], v221 offset:32768\n"
    "ds_read_b64_tr_b16 v[110:111], v221 offset:33024\n"
    "ds_read_b64_tr_b16 v[112:113], v221 offset:36864\n"
    "ds_read_b64_tr_b16 v[114:115], v221 offset:37120\n"
    "ds_read_b64_tr_b16 v[116:117], v221 offset:40960\n"
    "ds_read_b64_tr_b16 v[118:119], v221 offset:41216\n"
    "ds_read_b64_tr_b16 v[120:121], v221 offset:45056\n"
    "ds_read_b64_tr_b16 v[122:123], v221 offset:45312\n"
    "s_waitcnt lgkmcnt(8)\n"
    "v_mfma_f32_32x32x16_bf16 v[72:87], v[228:231], v[104:107], 0\n"
    "s_waitcnt lgkmcnt(6)\n"
    "v_mfma_f32_32x32x16_bf16 v[72:87], v[228:231], v[108:111], v[72:87]\n"
    "s_waitcnt lgkmcnt(4)\n"
    "v_mfma_f32_32x32x16_bf16 v[72:87], v[228:231], v[112:115], v[72:87]\n"
    "s_waitcnt lgkmcnt(2)\n"
    "v_mfma_f32_32x32x16_bf16 v[72:87], v[228:231], v[116:119], v[72:87]\n"
    "s_waitcnt lgkmcnt(0)\n"
    "v_mfma_f32_32x32x16_bf16 v[72:87], v[228:231], v[120:123], v[72:87]\n"
    "s_add_u32 m0, s74, 0xe000\n"
    "s_mov_b32 s56, s55\n"
    "buffer_load_dwordx4 v210, s[8:11], s56 offen lds\n"
    "s_add_u32 m0, s74, 0xf000\n"
    "s_add_u32 s56, s55, 0x2a000\n"
    "buffer_load_dwordx4 v210, s[8:11], s56 offen lds\n"
    "v_lshl_add_u32 v3, v198, 3, v199\n"
    "v_mul_u32_u24 v3, 0xc8, v3\n"
    "v_lshl_add_u32 v2, v200, 3, v3\n"
    "s_lshr_b32 s56, s40, 1\n"
    "s_lshl_b32 s57, s56, 6\n"
    "s_add_u32 s57, s57, 0x18000\n"
    "v_add_u32 v8, s57, v2\n"
    "s_and_b32 s58, s40, 1\n"
    "s_lshl_b32 s57, s58, 5\n"
    "s_add_u32 s57, s57, 0x18080\n"
    "v_add_u32 v9, s57, v2\n"
    "v_and_b32 v2, 1, v198\n"
    "v_lshlrev_b32 v2, 9, v2\n"
    "v_lshl_add_u32 v2, v197, 4, v2\n"
    "v_lshl_add_u32 v10, v201, 10, v2\n"
    "s_lshl_b32 s56, s56, 11\n"
    "s_lshl_b32 s58, s58, 8\n"
    "s_add_u32 s56, s56, s58\n"
    "s_lshl4_add_u32 s57, s42, s41\n"
    "s_lshl_b32 s57, s57, 12\n"
    "s_add_u32 s56, s56, s57\n"
    "s_add_u32 s60, s56, 0x10000\n"
    "s_mov_b32 s76, s74\n"
    "s_cmp_lt_u32 s44, 7\n"
    "s_cselect_b32 s63, 1, 0\n"
    "s_cselect_b32 s44, s44, 0\n"
    "s_lshl_b32 s56, s44, 9\n"
    "s_mov_b32 s79, s40\n"
    "s_lshl_b32 s57, s79, 6\n"
    "s_add_u32 s74, s56, s57\n"
    "s_lshl_b32 s45, s40, 13\n"
    "s_add_u32 s45, s45, 0x18000\n"
    "v_lshrrev_b32 v2, 2, v196\n"
    "v_mul_u32_u24 v2, 0xe00, v2\n"
    "v_lshl_add_u32 v245, v200, 4, v2\n"
    "v_and_b32 v4, 1, v198\n"
    "v_lshlrev_b32 v2, 2, v4\n"
    "v_and_b32 v3, 7, v196\n"
    "v_xor_b32 v2, v2, v3\n"
    "v_lshrrev_b32 v3, 3, v196\n"
    "v_mul_u32_u24 v3, 0x380, v3\n"
    "v_lshl_add_u32 v244, v2, 4, v3\n"
    "v_lshl_add_u32 v5, v201, 2, v199\n"
    "v_lshlrev_b32 v3, 3, v200\n"
    "v_lshl_add_u32 v3, v4, 5, v3\n"
    "v_lshl_add_u32 v246, v5, 6, v3\n"
    "v_add_u32 v246, s45, v246\n"
    "v_lshlrev_b32 v247, 3, v197\n"
    "s_barrier\n"
    "ds_read_b64_tr_b16 v[124:125], v221 offset:49152\n"
    "ds_read_b64_tr_b16 v[126:127], v221 offset:49408\n"
    "ds_read_b64_tr_b16 v[128:129], v221 offset:53248\n"
    "ds_read_b64_tr_b16 v[130:131], v221 offset:53504\n"
    "s_lshl_b32 s56, s61, 7\n"
    "v_add_u32 v221, s56, v215\n"
    "s_waitcnt lgkmcnt(2)\n"
    "v_mfma_f32_32x32x16_bf16 v[72:87], v[228:231], v[124:127], v[72:87]\n"
    "s_waitcnt lgkmcnt(0)\n"
    "v_mfma_f32_32x32x16_bf16 v[72:87], v[228:231], v[128:131], v[72:87]\n"
    "s_nop 11\n"
    "ds_write_b32 v221, v72\n"
    "s_waitcnt lgkmcnt(0)\n"
    "s_barrier\n"
    "ds_read_b64_tr_b16 v[32:33], v8\n"
    "ds_read_b64_tr_b16 v[34:35], v8 offset:800\n"
    "ds_read_b64_tr_b16 v[48:49], v8 offset:32\n"
    "ds_read_b64_tr_b16 v[50:51], v8 offset:832\n"
    "ds_read_b64_tr_b16 v[36:37], v8 offset:6400\n"
    "ds_read_b64_tr_b16 v[38:39], v8 offset:7200\n"
    "ds_read_b64_tr_b16 v[52:53], v8 offset:6432\n"
    "ds_read_b64_tr_b16 v[54:55], v8 offset:7232\n"
    "ds_read_b64_tr_b16 v[40:41], v8 offset:12800\n"
    "ds_read_b64_tr_b16 v[42:43], v8 offset:13600\n"
    "ds_read_b64_tr_b16 v[56:57], v8 offset:12832\n"
    "ds_read_b64_tr_b16 v[58:59], v8 offset:13632\n"
    "ds_read_b64_tr_b16 v[44:45], v8 offset:19200\n"
    "ds_read_b64_tr_b16 v[46:47], v8 offset:20000\n"
    "ds_read_b64_tr_b16 v[60:61], v8 offset:19232\n"
    "ds_read_b64_tr_b16 v[62:63], v8 offset:20032\n"
    "s_add_u32 m0, s76, 0x10000\n"
    "s_add_u32 s56, s55, 0x54000\n"
    "buffer_load_dwordx4 v210, s[8:11], s56 offen lds\n"
    "s_add_u32 m0, s76, 0x11000\n"
    "s_add_u32 s56, s55, 0x7e000\n"
    "buffer_load_dwordx4 v210, s[8:11], s56 offen lds\n"
    "s_add_u32 m0, s76, 0x12000\n"
    "s_add_u32 s56, s55, 0xa8000\n"
    "buffer_load_dwordx4 v210, s[8:11], s56 offen lds\n"
    "s_add_u32 m0, s76, 0x13000\n"
    "s_add_u32 s56, s55, 0xd2000\n"
    "buffer_load_dwordx4 v210, s[8:11], s56 offen lds\n"
    "s_add_u32 m0, s76, 0x14000\n"
    "s_add_u32 s56, s55, 0xfc000\n"
    "buffer_load_dwordx4 v210, s[8:11], s56 offen lds\n"
    "s_waitcnt vmcnt(4)\n"
    "s_waitcnt lgkmcnt(0)\n"
    "s_barrier\n"
    "ds_read_b64_tr_b16 v[16:17], v9\n"
    "ds_read_b64_tr_b16 v[18:19], v9 offset:800\n"
    "ds_read_b64_tr_b16 v[20:21], v9 offset:6400\n"
    "ds_read_b64_tr_b16 v[22:23], v9 offset:7200\n"
    "ds_read_b64_tr_b16 v[24:25], v9 offset:12800\n"
    "ds_read_b64_tr_b16 v[26:27], v9 offset:13600\n"
    "ds_read_b64_tr_b16 v[28:29], v9 offset:19200\n"
    "ds_read_b64_tr_b16 v[30:31], v9 offset:20000\n"
    "s_waitcnt lgkmcnt(6)\n"
    "v_mfma_f32_16x16x32_bf16 v[64:67], v[32:35], v[16:19], 0\n"
    "v_mfma_f32_16x16x32_bf16 v[68:71], v[48:51], v[16:19], 0\n"
    "s_waitcnt lgkmcnt(4)\n"
    "v_mfma_f32_16x16x32_bf16 v[64:67], v[36:39], v[20:23], v[64:67]\n"
    "v_mfma_f32_16x16x32_bf16 v[68:71], v[52:55], v[20:23], v[68:71]\n"
    "s_waitcnt lgkmcnt(2)\n"
    "v_mfma_f32_16x16x32_bf16 v[64:67], v[40:43], v[24:27], v[64:67]\n"
    "v_mfma_f32_16x16x32_bf16 v[68:71], v[56:59], v[24:27], v[68:71]\n"
    "s_waitcnt lgkmcnt(0)\n"
    "v_mfma_f32_16x16x32_bf16 v[64:67], v[44:47], v[28:31], v[64:67]\n"
    "v_mfma_f32_16x16x32_bf16 v[68:71], v[60:63], v[28:31], v[68:71]\n"
    "s_mul_i32 s57, s61, 100352\n"
    "s_lshl_b32 s58, s44, 7\n"
    "s_add_u32 s57, s57, s58\n"
    "s_mul_i32 s59, s61, 14336\n"
    "s_nop 2\n"
    "v_cvt_pk_bf16_f32 v72, v64, v65\n"
    "v_cvt_pk_bf16_f32 v73, v66, v67\n"
    "v_cvt_pk_bf16_f32 v74, v68, v69\n"
    "v_cvt_pk_bf16_f32 v75, v70, v71\n"
    "s_nop 0\n"
    "v_permlane32_swap_b32 v72, v74\n"
    "v_permlane32_swap_b32 v73, v75\n"
    "buffer_store_dwordx4 v[72:75], v10, s[12:15], s60 offen sc1\n"
    "s_waitcnt vmcnt(1)\n"
    "s_barrier\n"
    "s_mov_b32 m0, s59\n"
    "s_mov_b32 s56, s57\n"
    "buffer_load_dwordx4 v244, s[28:31], s56 offen lds\n"
    "s_addk_i32 m0, 0x400\n"
    "s_add_u32 s56, s56, 0x1c00\n"
    "buffer_load_dwordx4 v244, s[28:31], s56 offen lds\n"
    "s_addk_i32 m0, 0x400\n"
    "s_add_u32 s56, s56, 0x1c00\n"
    "buffer_load_dwordx4 v244, s[28:31], s56 offen lds\n"
    "s_addk_i32 m0, 0x400\n"
    "s_add_u32 s56, s56, 0x1c00\n"
    "buffer_load_dwordx4 v244, s[28:31], s56 offen lds\n"
    "s_addk_i32 m0, 0x400\n"
    "s_add_u32 s56, s56, 0x1c00\n"
    "buffer_load_dwordx4 v244, s[28:31], s56 offen lds\n"
    "s_addk_i32 m0, 0x400\n"
    "s_add_u32 s56, s56, 0x1c00\n"
    "buffer_load_dwordx4 v244, s[28:31], s56 offen lds\n"
    "s_lshl_b32 s60, s42, 8\n"
    "s_lshl3_add_u32 s58, s41, s60\n"
    "s_waitcnt vmcnt(6)\n"
    "v_mov_b32 v2, 1\n"
    "v_mov_b32 v3, 0x17200\n"
    "s_mov_b64 exec, 1\n"
    "ds_add_rtn_u32 v3, v3, v2\n"
    "s_waitcnt lgkmcnt(0)\n"
    "v_readfirstlane_b32 s56, v3\n"
    "s_cmp_lg_u32 s56, 3\n"
    "s_cbranch_scc1 .Lfb_s410\n"
    "s_add_u32 s56, s58, 0x1000\n"
    "buffer_store_dwordx2 v[6:7], off, s[12:15], s56 sc1\n"
    ".Lfb_s410:\n"
    "s_mov_b64 exec, -1\n"
    "s_add_u32 m0, s59, 0x1800\n"
    "s_add_u32 s56, s57, 0xa800\n"
    "buffer_load_dwordx4 v244, s[28:31], s56 offen lds\n"
    "s_addk_i32 m0, 0x400\n"
    "s_add_u32 s56, s56, 0x1c00\n"
    "buffer_load_dwordx4 v244, s[28:31], s56 offen lds\n"
    "s_addk_i32 m0, 0x400\n"
    "s_add_u32 s56, s56, 0x1c00\n"
    "buffer_load_dwordx4 v244, s[28:31], s56 offen lds\n"
    "s_addk_i32 m0, 0x400\n"
    "s_add_u32 s56, s56, 0x1c00\n"
    "buffer_load_dwordx4 v244, s[28:31], s56 offen lds\n"
    "s_addk_i32 m0, 0x400\n"
    "s_add_u32 s56, s56, 0x1c00\n"
    "buffer_load_dwordx4 v244, s[28:31], s56 offen lds\n"
    "s_addk_i32 m0, 0x400\n"
    "s_add_u32 s56, s56, 0x1c00\n"
    "buffer_load_dwordx4 v244, s[28:31], s56 offen lds\n"
    "s_addk_i32 m0, 0x400\n"
    "s_add_u32 s56, s56, 0x1c00\n"
    "buffer_load_dwordx4 v244, s[28:31], s56 offen lds\n"
    "s_addk_i32 m0, 0x400\n"
    "s_add_u32 s56, s56, 0x1c00\n"
    "buffer_load_dwordx4 v244, s[28:31], s56 offen lds\n"
    "s_barrier\n"
    "s_mov_b32 m0, s45\n"
    "s_mov_b32 s56, s74\n"
    "buffer_load_dwordx4 v245, s[20:23], s56 offen lds\n"
    "s_addk_i32 m0, 0x400\n"
    "s_add_u32 s56, s56, 0xe000\n"
    "buffer_load_dwordx4 v245, s[20:23], s56 offen lds\n"
    "s_addk_i32 m0, 0x400\n"
    "s_add_u32 s56, s56, 0xe000\n"
    "buffer_load_dwordx4 v245, s[20:23], s56 offen lds\n"
    "s_addk_i32 m0, 0x400\n"
    "s_add_u32 s56, s56, 0xe000\n"
    "buffer_load_dwordx4 v245, s[20:23], s56 offen lds\n"
    "s_addk_i32 m0, 0x400\n"
    "s_add_u32 s56, s56, 0xe000\n"
    "buffer_load_dwordx4 v245, s[20:23], s56 offen lds\n"
    "s_addk_i32 m0, 0x400\n"
    "s_add_u32 s56, s56, 0xe000\n"
    "buffer_load_dwordx4 v245, s[20:23], s56 offen lds\n"
    "s_addk_i32 m0, 0x400\n"
    "s_add_u32 s56, s56, 0xe000\n"
    "buffer_load_dwordx4 v245, s[20:23], s56 offen lds\n"
    "s_addk_i32 m0, 0x400\n"
    "s_add_u32 s56, s56, 0xe000\n"
    "buffer_load_dwordx4 v245, s[20:23], s56 offen lds\n"
    "s_add_u32 m0, s45, 0x8000\n"
    "s_add_u32 s56, s74, 0x100\n"
    "buffer_load_dwordx4 v245, s[20:23], s56 offen lds\n"
    "s_addk_i32 m0, 0x400\n"
    "s_add_u32 s56, s56, 0xe000\n"
    "buffer_load_dwordx4 v245, s[20:23], s56 offen lds\n"
    "s_addk_i32 m0, 0x400\n"
    "s_add_u32 s56, s56, 0xe000\n"
    "buffer_load_dwordx4 v245, s[20:23], s56 offen lds\n"
    "s_addk_i32 m0, 0x400\n"
    "s_add_u32 s56, s56, 0xe000\n"
    "buffer_load_dwordx4 v245, s[20:23], s56 offen lds\n"
    "s_addk_i32 m0, 0x400\n"
    "s_add_u32 s56, s56, 0xe000\n"
    "buffer_load_dwordx4 v245, s[20:23], s56 offen lds\n"
    "s_addk_i32 m0, 0x400\n"
    "s_add_u32 s56, s56, 0xe000\n"
    "buffer_load_dwordx4 v245, s[20:23], s56 offen lds\n"
    "s_addk_i32 m0, 0x400\n"
    "s_add_u32 s56, s56, 0xe000\n"
    "buffer_load_dwordx4 v245, s[20:23], s56 offen lds\n"
    "s_addk_i32 m0, 0x400\n"
    "s_add_u32 s56, s56, 0xe000\n"
    "buffer_load_dwordx4 v245, s[20:23], s56 offen lds\n"
    "v_lshlrev_b32 v2, 4, v197\n"
    "v_lshl_add_u32 v164, v198, 12, v2\n"
    "v_lshl_add_u32 v165, v198, 3, v2\n"
    "s_lshl_b32 s56, s40, 9\n"
    "s_add_u32 s57, s56, 0x15000\n"
    "v_add_u32 v165, s57, v165\n"
    "v_add_u32 v166, 0x15000, v1\n"
    "v_and_b32 v2, 1, v196\n"
    "v_lshlrev_b32 v2, 4, v2\n"
    "v_mov_b32 v3, 0x3f80\n"
    "v_lshlrev_b32 v2, v2, v3\n"
    "v_lshrrev_b32 v3, 1, v197\n"
    "v_cmp_eq_u32 vcc, 0, v3\n"
    "s_nop 1\n"
    "v_cndmask_b32 v160, 0, v2, vcc\n"
    "v_cmp_eq_u32 vcc, 1, v3\n"
    "s_nop 1\n"
    "v_cndmask_b32 v161, 0, v2, vcc\n"
    "v_cmp_eq_u32 vcc, 2, v3\n"
    "s_nop 1\n"
    "v_cndmask_b32 v162, 0, v2, vcc\n"
    "v_cmp_eq_u32 vcc, 3, v3\n"
    "s_nop 1\n"
    "v_cndmask_b32 v163, 0, v2, vcc\n"
    "s_lshl_b32 s59, s54, 16\n"
    "s_add_u32 s59, s59, s56\n"
    "s_lshl_b32 s57, s54, 8\n"
    "s_cmp_lg_u32 s40, 0\n"
    "s_cbranch_scc1 .Lfb_np11\n"
    "s_add_u32 s56, s57, 0x1000\n"
    "s_mov_b32 s64, 0x30d40\n"
    ".Lfb_pl12:\n"
    "buffer_load_dwordx2 v[2:3], v247, s[12:15], s56 offen sc1\n"
    "s_waitcnt vmcnt(0)\n"
    "v_cmp_ne_u64 vcc, v[2:3], s[46:47]\n"
    "s_cbranch_vccz .Lfb_np11\n"
    "s_sleep 1\n"
    "s_sub_u32 s64, s64, 1\n"
    "s_cbranch_scc0 .Lfb_pl12\n"
    ".Lfb_np11:\n"
    "s_branch .Lfb_rj5\n"
    ".Lfb_rv4:\n"
    "s_mov_b32 m0, s74\n"
    "s_add_u32 s56, s73, 0x380\n"
    "s_waitcnt lgkmcnt(0)\n"
    "buffer_load_dwordx4 v210, s[8:11], s56 offen lds\n"
    "s_add_u32 m0, s74, 0x1000\n"
    "s_add_u32 s56, s73, 0x2a380\n"
    "buffer_load_dwordx4 v210, s[8:11], s56 offen lds\n"
    "s_add_u32 m0, s74, 0x2000\n"
    "s_add_u32 s56, s73, 0x54380\n"
    "buffer_load_dwordx4 v210, s[8:11], s56 offen lds\n"
    "s_barrier\n"
    "s_add_u32 m0, s74, 0x3000\n"
    "s_add_u32 s56, s73, 0x7e380\n"
    "buffer_load_dwordx4 v210, s[8:11], s56 offen lds\n"
    "s_add_u32 m0, s74, 0x4000\n"
    "s_add_u32 s56, s73, 0xa8380\n"
    "buffer_load_dwordx4 v210, s[8:11], s56 offen lds\n"
    "s_add_u32 m0, s74, 0x5000\n"
    "s_add_u32 s56, s73, 0xd2380\n"
    "buffer_load_dwordx4 v210, s[8:11], s56 offen lds\n"
    "s_add_u32 m0, s74, 0x6000\n"
    "s_add_u32 s56, s73, 0xfc380\n"
    "buffer_load_dwordx4 v210, s[8:11], s56 offen lds\n"
    "s_mov_b32 m0, s45\n"
    "s_mov_b32 s56, s75\n"
    "buffer_load_dwordx4 v211, s[4:7], s56 offen lds\n"
    "buffer_load_dwordx4 v212, s[4:7], s56 offen offset:1024 lds\n"
    "s_add_u32 s56, s75, 0x3000\n"
    "buffer_load_dwordx4 v211, s[4:7], s56 offen offset:2048 lds\n"
    "buffer_load_dwordx4 v212, s[4:7], s56 offen offset:3072 lds\n"
    "buffer_load_dwordx2 v[218:219], off, s[12:15], 0 sc1\n"
    "v_lshrrev_b32 v201, 5, v196\n"
    "v_and_b32 v197, 15, v196\n"
    "v_and_b32 v202, 31, v196\n"
    "v_bfe_u32 v199, v196, 2, 2\n"
    "v_bfe_u32 v2, v196, 1, 3\n"
    "v_xor_b32 v2, v2, v201\n"
    "v_lshlrev_b32 v2, 4, v2\n"
    "v_lshl_add_u32 v2, v202, 7, v2\n"
    "v_add_u32 v204, s45, v2\n"
    "v_xor_b32 v205, 0x20, v204\n"
    "v_xor_b32 v206, 0x40, v204\n"
    "v_xor_b32 v207, 0x60, v204\n"
    "v_and_b32 v4, 1, v198\n"
    "v_lshl_add_u32 v4, v4, 2, v200\n"
    "v_lshl_add_u32 v3, v201, 3, v199\n"
    "s_and_b32 s56, s42, 1\n"
    "v_add_lshl_u32 v2, v4, s56, 3\n"
    "v_and_b32 v2, 63, v2\n"
    "v_lshl_add_u32 v208, v3, 6, v2\n"
    "s_and_b32 s56, s54, 1\n"
    "v_add_lshl_u32 v2, v4, s56, 3\n"
    "v_and_b32 v2, 63, v2\n"
    "v_lshl_add_u32 v209, v3, 6, v2\n"
    "v_add_u32 v209, 0xe000, v209\n"
    "v_mov_b32 v228, 0x3f803f80\n"
    "v_mov_b32 v229, 0x3f803f80\n"
    "v_mov_b32 v230, 0x3f803f80\n"
    "v_mov_b32 v231, 0x3f803f80\n"
    "v_cmp_gt_u32 s[50:51], 32, v196\n"
    "v_cmp_le_u32 s[52:53], 32, v196\n"
    "s_lshl_b32 s56, s61, 5\n"
    "v_add_u32 v2, s56, v202\n"
    "v_mul_u32_u24 v213, 0xc8, v2\n"
    "v_lshl_add_u32 v213, v201, 3, v213\n"
    "v_add_u32 v213, 0x18000, v213\n"
    "v_lshlrev_b32 v220, 4, v2\n"
    "v_add_u32 v220, 0x16200, v220\n"
    "v_lshlrev_b32 v215, 2, v202\n"
    "v_add_u32 v215, 0x16000, v215\n"
    "v_mov_b32 v234, 0\n"
    "v_mov_b32 v235, 0\n"
    "s_waitcnt vmcnt(5)\n"
    "s_barrier\n"
    "s_add_u32 m0, s45, 0x4000\n"
    "s_add_u32 s56, s75, 0x80\n"
    "buffer_load_dwordx4 v211, s[4:7], s56 offen lds\n"
    "buffer_load_dwordx4 v212, s[4:7], s56 offen offset:1024 lds\n"
    "s_add_u32 s56, s75, 0x3080\n"
    "buffer_load_dwordx4 v211, s[4:7], s56 offen offset:2048 lds\n"
    "buffer_load_dwordx4 v212, s[4:7], s56 offen offset:3072 lds\n"
    "s_add_u32 m0, s45, 0x8000\n"
    "s_add_u32 s56, s75, 0x100\n"
    "buffer_load_dwordx4 v211, s[4:7], s56 offen lds\n"
    "buffer_load_dwordx4 v212, s[4:7], s56 offen offset:1024 lds\n"
    "s_add_u32 s56, s75, 0x3100\n"
    "buffer_load_dwordx4 v211, s[4:7], s56 offen offset:2048 lds\n"
    "buffer_load_dwordx4 v212, s[4:7], s56 offen offset:3072 lds\n"
    "s_waitcnt vmcnt(8)\n"
    "s_barrier\n"
    "s_add_u32 m0, s45, 0xc000\n"
    "s_add_u32 s56, s75, 0x180\n"
    "buffer_load_dwordx4 v211, s[4:7], s56 offen lds\n"
    "buffer_load_dwordx4 v212, s[4:7], s56 offen offset:1024 lds\n"
    "ds_read_b128 v[8:11], v204\n"
    "ds_read_b128 v[12:15], v205\n"
    "ds_read_b128 v[16:19], v206\n"
    "ds_read_b128 v[20:23], v207\n"
    "s_waitcnt vmcnt(6)\n"
    "s_waitcnt lgkmcnt(0)\n"
    "s_barrier\n"
    "s_mov_b32 m0, s45\n"
    "s_add_u32 s56, s75, 0x200\n"
    "buffer_load_dwordx4 v211, s[4:7], s56 offen lds\n"
    "buffer_load_dwordx4 v212, s[4:7], s56 offen offset:1024 lds\n"
    "ds_read_b128 v[24:27], v204 offset:16384\n"
    "ds_read_b128 v[28:31], v205 offset:16384\n"
    "ds_read_b128 v[32:35], v206 offset:16384\n"
    "ds_read_b128 v[36:39], v207 offset:16384\n"
    "s_waitcnt vmcnt(4)\n"
    "s_waitcnt lgkmcnt(0)\n"
    "s_barrier\n"
    "s_add_u32 m0, s45, 0x4000\n"
    "s_add_u32 s56, s75, 0x280\n"
    "buffer_load_dwordx4 v211, s[4:7], s56 offen lds\n"
    "buffer_load_dwordx4 v212, s[4:7], s56 offen offset:1024 lds\n"
    "ds_read_b128 v[40:43], v204 offset:32768\n"
    "ds_read_b128 v[44:47], v205 offset:32768\n"
    "ds_read_b128 v[48:51], v206 offset:32768\n"
    "ds_read_b128 v[52:55], v207 offset:32768\n"
    "s_waitcnt vmcnt(4)\n"
    "s_waitcnt lgkmcnt(0)\n"
    "s_barrier\n"
    "s_add_u32 m0, s45, 0x8000\n"
    "s_add_u32 s56, s75, 0x300\n"
    "buffer_load_dwordx4 v211, s[4:7], s56 offen lds\n"
    "buffer_load_dwordx4 v212, s[4:7], s56 offen offset:1024 lds\n"
    "ds_read_b128 v[56:59], v204 offset:49152\n"
    "ds_read_b128 v[60:63], v205 offset:49152\n"
    "ds_read_b128 v[64:67], v206 offset:49152\n"
    "ds_read_b128 v[68:71], v207 offset:49152\n"
    "s_waitcnt vmcnt(4)\n"
    "s_waitcnt lgkmcnt(0)\n"
    "s_barrier\n"
    "s_add_u32 m0, s74, 0x7000\n"
    "s_add_u32 s56, s73, 0x700\n"
    "buffer_load_dwordx4 v210, s[8:11], s56 offen lds\n"
    "s_add_u32 m0, s74, 0x8000\n"
    "s_add_u32 s56, s73, 0x2a700\n"
    "buffer_load_dwordx4 v210, s[8:11], s56 offen lds\n"
    "ds_read_b128 v[72:75], v204\n"
    "ds_read_b128 v[76:79], v205\n"
    "ds_read_b128 v[80:83], v206\n"
    "ds_read_b128 v[84:87], v207\n"
    "s_waitcnt vmcnt(4)\n"
    "s_waitcnt lgkmcnt(0)\n"
    "s_barrier\n"
    "s_add_u32 m0, s74, 0x9000\n"
    "s_add_u32 s56, s73, 0x54700\n"
    "buffer_load_dwordx4 v210, s[8:11], s56 offen lds\n"
    "s_add_u32 m0, s74, 0xa000\n"
    "s_add_u32 s56, s73, 0x7e700\n"
    "buffer_load_dwordx4 v210, s[8:11], s56 offen lds\n"
    "ds_read_b128 v[88:91], v204 offset:16384\n"
    "ds_read_b128 v[92:95], v205 offset:16384\n"
    "ds_read_b128 v[96:99], v206 offset:16384\n"
    "ds_read_b128 v[100:103], v207 offset:16384\n"
    "s_waitcnt vmcnt(4)\n"
    "s_waitcnt lgkmcnt(0)\n"
    "s_barrier\n"
    "ds_read_b128 v[104:107], v204 offset:32768\n"
    "ds_read_b128 v[108:111], v205 offset:32768\n"
    "ds_read_b128 v[112:115], v206 offset:32768\n"
    "ds_read_b128 v[116:119], v207 offset:32768\n"
    "s_add_u32 m0, s74, 0xb000\n"
    "s_add_u32 s56, s73, 0xa8700\n"
    "buffer_load_dwordx4 v210, s[8:11], s56 offen lds\n"
    "s_add_u32 m0, s74, 0xc000\n"
    "s_add_u32 s56, s73, 0xd2700\n"
    "buffer_load_dwordx4 v210, s[8:11], s56 offen lds\n"
    "s_add_u32 m0, s74, 0xd000\n"
    "s_add_u32 s56, s73, 0xfc700\n"
    "buffer_load_dwordx4 v210, s[8:11], s56 offen lds\n"
    "v_readfirstlane_b32 s46, v218\n"
    "v_readfirstlane_b32 s47, v219\n"
    "s_add_u32 s46, s46, 1\n"
    "s_addc_u32 s47, s47, 0\n"
    "v_mov_b32 v6, s46\n"
    "v_mov_b32 v7, s47\n"
    "s_waitcnt vmcnt(2)\n"
    "s_barrier\n"
    "ds_read_b128 v[236:239], v220\n"
    "ds_read_b64_tr_b16 v[120:121], v208 offset:28672\n"
    "ds_read_b64_tr_b16 v[122:123], v208 offset:28928\n"
    "ds_read_b64_tr_b16 v[124:125], v208 offset:29696\n"
    "ds_read_b64_tr_b16 v[126:127], v208 offset:29952\n"
    "ds_read_b64_tr_b16 v[128:129], v208 offset:30720\n"
    "ds_read_b64_tr_b16 v[130:131], v208 offset:30976\n"
    "s_waitcnt lgkmcnt(4)\n"
    "v_mfma_f32_32x32x16_bf16 v[136:151], v[120:123], v[8:11], 0\n"
    "ds_read_b64_tr_b16 v[132:133], v208 offset:31744\n"
    "ds_read_b64_tr_b16 v[134:135], v208 offset:32000\n"
    "s_waitcnt lgkmcnt(4)\n"
    "v_mfma_f32_32x32x16_bf16 v[136:151], v[124:127], v[12:15], v[136:151]\n"
    "ds_read_b64_tr_b16 v[120:121], v208 offset:32768\n"
    "ds_read_b64_tr_b16 v[122:123], v208 offset:33024\n"
    "s_waitcnt lgkmcnt(4)\n"
    "v_mfma_f32_32x32x16_bf16 v[136:151], v[128:131], v[16:19], v[136:151]\n"
    "ds_read_b64_tr_b16 v[124:125], v208 offset:33792\n"
    "ds_read_b64_tr_b16 v[126:127], v208 offset:34048\n"
    "v_mov_b32 v226, v239\n"
    "v_mov_b32 v239, 0\n"
    "v_mul_f32 v224, 0x3fb8aa3b, v226\n"
    "s_waitcnt lgkmcnt(4)\n"
    "v_mfma_f32_32x32x16_bf16 v[136:151], v[132:135], v[20:23], v[136:151]\n"
    "ds_read_b64_tr_b16 v[128:129], v208 offset:34816\n"
    "ds_read_b64_tr_b16 v[130:131], v208 offset:35072\n"
    "s_waitcnt lgkmcnt(4)\n"
    "v_mfma_f32_32x32x16_bf16 v[136:151], v[120:123], v[24:27], v[136:151]\n"
    "ds_read_b64_tr_b16 v[132:133], v208 offset:35840\n"
    "ds_read_b64_tr_b16 v[134:135], v208 offset:36096\n"
    "s_waitcnt lgkmcnt(4)\n"
    "v_mfma_f32_32x32x16_bf16 v[136:151], v[124:127], v[28:31], v[136:151]\n"
    "ds_read_b64_tr_b16 v[120:121], v208 offset:36864\n"
    "ds_read_b64_tr_b16 v[122:123], v208 offset:37120\n"
    "s_waitcnt lgkmcnt(4)\n"
    "v_mfma_f32_32x32x16_bf16 v[136:151], v[128:131], v[32:35], v[136:151]\n"
    "ds_read_b64_tr_b16 v[124:125], v208 offset:37888\n"
    "ds_read_b64_tr_b16 v[126:127], v208 offset:38144\n"
    "s_waitcnt lgkmcnt(4)\n"
    "v_mfma_f32_32x32x16_bf16 v[136:151], v[132:135], v[36:39], v[136:151]\n"
    "ds_read_b64_tr_b16 v[128:129], v208 offset:38912\n"
    "ds_read_b64_tr_b16 v[130:131], v208 offset:39168\n"
    "s_waitcnt lgkmcnt(4)\n"
    "v_mfma_f32_32x32x16_bf16 v[136:151], v[120:123], v[40:43], v[136:151]\n"
    "ds_read_b64_tr_b16 v[132:133], v208 offset:39936\n"
    "ds_read_b64_tr_b16 v[134:135], v208 offset:40192\n"
    "s_waitcnt lgkmcnt(4)\n"
    "v_mfma_f32_32x32x16_bf16 v[136:151], v[124:127], v[44:47], v[136:151]\n"
    "ds_read_b64_tr_b16 v[120:121], v208 offset:40960\n"
    "ds_read_b64_tr_b16 v[122:123], v208 offset:41216\n"
    "s_waitcnt lgkmcnt(4)\n"
    "v_mfma_f32_32x32x16_bf16 v[136:151], v[128:131], v[48:51], v[136:151]\n"
    "ds_read_b64_tr_b16 v[124:125], v208 offset:41984\n"
    "ds_read_b64_tr_b16 v[126:127], v208 offset:42240\n"
    "s_waitcnt lgkmcnt(4)\n"
    "v_mfma_f32_32x32x16_bf16 v[136:151], v[132:135], v[52:55], v[136:151]\n"
    "ds_read_b64_tr_b16 v[128:129], v208 offset:43008\n"
    "ds_read_b64_tr_b16 v[130:131], v208 offset:43264\n"
    "s_waitcnt lgkmcnt(4)\n"
    "v_mfma_f32_32x32x16_bf16 v[136:151], v[120:123], v[56:59], v[136:151]\n"
    "ds_read_b64_tr_b16 v[132:133], v208 offset:44032\n"
    "ds_read_b64_tr_b16 v[134:135], v208 offset:44288\n"
    "s_waitcnt lgkmcnt(4)\n"
    "v_mfma_f32_32x32x16_bf16 v[136:151], v[124:127], v[60:63], v[136:151]\n"
    "ds_read_b64_tr_b16 v[120:121], v208 offset:45056\n"
    "ds_read_b64_tr_b16 v[122:123], v208 offset:45312\n"
    "s_waitcnt lgkmcnt(4)\n"
    "v_mfma_f32_32x32x16_bf16 v[136:151], v[128:131], v[64:67], v[136:151]\n"
    "ds_read_b64_tr_b16 v[124:125], v208 offset:46080\n"
    "ds_read_b64_tr_b16 v[126:127], v208 offset:46336\n"
    "s_waitcnt lgkmcnt(4)\n"
    "v_mfma_f32_32x32x16_bf16 v[136:151], v[132:135], v[68:71], v[136:151]\n"
    "ds_read_b64_tr_b16 v[128:129], v208 offset:47104\n"
    "ds_read_b64_tr_b16 v[130:131], v208 offset:47360\n"
    "s_waitcnt lgkmcnt(4)\n"
    "v_mfma_f32_32x32x16_bf16 v[136:151], v[120:123], v[72:75], v[136:151]\n"
    "ds_read_b64_tr_b16 v[132:133], v208 offset:48128\n"
    "ds_read_b64_tr_b16 v[134:135], v208 offset:48384\n"
    "s_waitcnt vmcnt(0)\n"
    "s_barrier\n"
    "s_waitcnt lgkmcnt(4)\n"
    "v_mfma_f32_32x32x16_bf16 v[136:151], v[124:127], v[76:79], v[136:151]\n"
    "ds_read_b64_tr_b16 v[120:121], v208 offset:49152\n"
    "ds_read_b64_tr_b16 v[122:123], v208 offset:49408\n"
    "s_waitcnt lgkmcnt(4)\n"
    "v_mfma_f32_32x32x16_bf16 v[136:151], v[128:131], v[80:83], v[136:151]\n"
    "ds_read_b64_tr_b16 v[124:125], v208 offset:50176\n"
    "ds_read_b64_tr_b16 v[126:127], v208 offset:50432\n"
    "s_waitcnt lgkmcnt(4)\n"
    "v_mfma_f32_32x32x16_bf16 v[136:151], v[132:135], v[84:87], v[136:151]\n"
    "ds_read_b64_tr_b16 v[128:129], v208 offset:51200\n"
    "ds_read_b64_tr_b16 v[130:131], v208 offset:51456\n"
    "s_waitcnt lgkmcnt(4)\n"
    "v_mfma_f32_32x32x16_bf16 v[136:151], v[120:123], v[88:91], v[136:151]\n"
    "ds_read_b64_tr_b16 v[132:133], v208 offset:52224\n"
    "ds_read_b64_tr_b16 v[134:135], v208 offset:52480\n"
    "s_waitcnt lgkmcnt(4)\n"
    "v_mfma_f32_32x32x16_bf16 v[136:151], v[124:127], v[92:95], v[136:151]\n"
    "ds_read_b64_tr_b16 v[120:121], v208 offset:53248\n"
    "ds_read_b64_tr_b16 v[122:123], v208 offset:53504\n"
    "s_waitcnt lgkmcnt(4)\n"
    "v_mfma_f32_32x32x16_bf16 v[136:151], v[128:131], v[96:99], v[136:151]\n"
    "ds_read_b64_tr_b16 v[124:125], v208 offset:54272\n"
    "ds_read_b64_tr_b16 v[126:127], v208 offset:54528\n"
    "s_waitcnt lgkmcnt(4)\n"
    "v_mfma_f32_32x32x16_bf16 v[136:151], v[132:135], v[100:103], v[136:151]\n"
    "ds_read_b64_tr_b16 v[128:129], v208 offset:55296\n"
    "ds_read_b64_tr_b16 v[130:131], v208 offset:55552\n"
    "s_waitcnt lgkmcnt(4)\n"
    "v_mfma_f32_32x32x16_bf16 v[136:151], v[120:123], v[104:107], v[136:151]\n"
    "ds_read_b64_tr_b16 v[132:133], v208 offset:56320\n"
    "ds_read_b64_tr_b16 v[134:135], v208 offset:56576\n"
    "s_waitcnt lgkmcnt(4)\n"
    "v_mfma_f32_32x32x16_bf16 v[136:151], v[124:127], v[108:111], v[136:151]\n"
    "s_barrier\n"
    "ds_read_b32 v152, v215\n"
    "ds_read_b32 v153, v215 offset:128\n"
    "ds_read_b32 v154, v215 offset:256\n"
    "ds_read_b32 v155, v215 offset:384\n"
    "s_waitcnt lgkmcnt(6)\n"
    "v_mfma_f32_32x32x16_bf16 v[136:151], v[128:131], v[112:115], v[136:151]\n"
    "s_waitcnt lgkmcnt(4)\n"
    "v_mfma_f32_32x32x16_bf16 v[136:151], v[132:135], v[116:119], v[136:151]\n"
    "s_waitcnt lgkmcnt(2)\n"
    "v_add_f32 v152, v152, v153\n"
    "s_waitcnt lgkmcnt(0)\n"
    "v_add_f32 v154, v154, v155\n"
    "v_sub_f32 v152, -v152, v154\n"
    "v_cvt_pk_bf16_f32 v232, v152, v152\n"
    "v_lshlrev_b32 v153, 16, v232\n"
    "v_sub_f32 v152, v152, v153\n"
    "v_cvt_pk_bf16_f32 v233, v152, 0\n"
    "v_cndmask_b32 v232, 0, v232, s[50:51]\n"
    "v_cndmask_b32 v233, 0, v233, s[50:51]\n"
    "s_nop 1\n"
    "v_mfma_f32_32x32x16_bf16 v[136:151], v[232:235], v[236:239], v[136:151]\n"
    "v_mov_b32 v3, 0x3f80\n"
    "s_nop 10\n"
    "v_pk_mul_f32 v[136:137], v[136:137], v[226:227] op_sel_hi:[1,0]\n"
    "v_pk_mul_f32 v[138:139], v[138:139], v[226:227] op_sel_hi:[1,0]\n"
    "v_pk_mul_f32 v[140:141], v[140:141], v[226:227] op_sel_hi:[1,0]\n"
    "v_pk_mul_f32 v[142:143], v[142:143], v[226:227] op_sel_hi:[1,0]\n"
    "v_pk_mul_f32 v[144:145], v[144:145], v[226:227] op_sel_hi:[1,0]\n"
    "v_pk_mul_f32 v[146:147], v[146:147], v[226:227] op_sel_hi:[1,0]\n"
    "v_pk_mul_f32 v[148:149], v[148:149], v[226:227] op_sel_hi:[1,0]\n"
    "v_pk_mul_f32 v[150:151], v[150:151], v[226:227] op_sel_hi:[1,0]\n"
    "v_cvt_pk_bf16_f32 v120, v136, v137\n"
    "v_cvt_pk_bf16_f32 v121, v138, v139\n"
    "v_cvt_pk_bf16_f32 v122, v140, v141\n"
    "v_cvt_pk_bf16_f32 v123, v142, v143\n"
    "v_cvt_pk_bf16_f32 v124, v144, v145\n"
    "v_cvt_pk_bf16_f32 v125, v146, v147\n"
    "v_cvt_pk_bf16_f32 v126, v148, v149\n"
    "v_cvt_pk_bf16_f32 v127, v150, v151\n"
    "v_cndmask_b32 v126, v126, v3, s[52:53]\n"
    "v_cndmask_b32 v127, v127, 0, s[52:53]\n"
    "ds_write2_b64 v213, v[120:121], v[122:123] offset0:16 offset1:18\n"
    "ds_write2_b64 v213, v[124:125], v[126:127] offset0:20 offset1:22\n"
    "s_waitcnt vmcnt(0) lgkmcnt(0)\n"
    "s_barrier\n"
    "v_add_u32 v2, 0x16a00, v1\n"
    "ds_read_b128 v[184:187], v2\n"
    "ds_read_b128 v[188:191], v2 offset:1024\n"
    "v_mov_b32 v192, v209\n"
    "v_mov_b32 v193, v211\n"
    "v_mov_b32 v194, v215\n"
    "ds_read_b64_tr_b16 v[120:121], v192\n"
    "ds_read_b64_tr_b16 v[122:123], v192 offset:256\n"
    "ds_read_b64_tr_b16 v[124:125], v192 offset:1024\n"
    "ds_read_b64_tr_b16 v[126:127], v192 offset:1280\n"
    "ds_read_b64_tr_b16 v[128:129], v192 offset:2048\n"
    "ds_read_b64_tr_b16 v[130:131], v192 offset:2304\n"
    "s_waitcnt lgkmcnt(4)\n"
    "v_mfma_f32_32x32x16_bf16 v[136:151], v[120:123], v[8:11], 0\n"
    "ds_read_b64_tr_b16 v[132:133], v192 offset:3072\n"
    "ds_read_b64_tr_b16 v[134:135], v192 offset:3328\n"
    "s_waitcnt lgkmcnt(4)\n"
    "v_mfma_f32_32x32x16_bf16 v[136:151], v[124:127], v[12:15], v[136:151]\n"
    "ds_read_b64_tr_b16 v[120:121], v192 offset:4096\n"
    "ds_read_b64_tr_b16 v[122:123], v192 offset:4352\n"
    "s_waitcnt lgkmcnt(4)\n"
    "v_mfma_f32_32x32x16_bf16 v[136:151], v[128:131], v[16:19], v[136:151]\n"
    "ds_read_b64_tr_b16 v[124:125], v192 offset:5120\n"
    "ds_read_b64_tr_b16 v[126:127], v192 offset:5376\n"
    "s_waitcnt lgkmcnt(4)\n"
    "v_mfma_f32_32x32x16_bf16 v[136:151], v[132:135], v[20:23], v[136:151]\n"
    "ds_read_b64_tr_b16 v[128:129], v192 offset:6144\n"
    "ds_read_b64_tr_b16 v[130:131], v192 offset:6400\n"
    "s_waitcnt lgkmcnt(4)\n"
    "v_mfma_f32_32x32x16_bf16 v[136:151], v[120:123], v[24:27], v[136:151]\n"
    "ds_read_b64_tr_b16 v[132:133], v192 offset:7168\n"
    "ds_read_b64_tr_b16 v[134:135], v192 offset:7424\n"
    "s_waitcnt lgkmcnt(4)\n"
    "v_mfma_f32_32x32x16_bf16 v[136:151], v[124:127], v[28:31], v[136:151]\n"
    "ds_read_b64_tr_b16 v[120:121], v192 offset:8192\n"
    "ds_read_b64_tr_b16 v[122:123], v192 offset:8448\n"
    "s_waitcnt lgkmcnt(4)\n"
    "v_mfma_f32_32x32x16_bf16 v[136:151], v[128:131], v[32:35], v[136:151]\n"
    "ds_read_b64_tr_b16 v[124:125], v192 offset:9216\n"
    "ds_read_b64_tr_b16 v[126:127], v192 offset:9472\n"
    "s_waitcnt lgkmcnt(4)\n"
    "v_mfma_f32_32x32x16_bf16 v[136:151], v[132:135], v[36:39], v[136:151]\n"
    "ds_read_b64_tr_b16 v[128:129], v192 offset:10240\n"
    "ds_read_b64_tr_b16 v[130:131], v192 offset:10496\n"
    "s_waitcnt lgkmcnt(4)\n"
    "v_mfma_f32_32x32x16_bf16 v[136:151], v[120:123], v[40:43], v[136:151]\n"
    "ds_read_b64_tr_b16 v[132:133], v192 offset:11264\n"
    "ds_read_b64_tr_b16 v[134:135], v192 offset:11520\n"
    "s_barrier\n"
    "s_lshl_b32 s56, s61, 10\n"
    "v_add_u32 v221, s56, v192\n"
    "ds_read_b64_tr_b16 v[152:153], v221\n"
    "ds_read_b64_tr_b16 v[154:155], v221 offset:256\n"
    "ds_read_b64_tr_b16 v[156:157], v221 offset:4096\n"
    "ds_read_b64_tr_b16 v[158:159], v221 offset:4352\n"
    "ds_read_b64_tr_b16 v[160:161], v221 offset:8192\n"
    "ds_read_b64_tr_b16 v[162:163], v221 offset:8448\n"
    "ds_read_b64_tr_b16 v[164:165], v221 offset:12288\n"
    "ds_read_b64_tr_b16 v[166:167], v221 offset:12544\n"
    "ds_read_b64_tr_b16 v[168:169], v221 offset:16384\n"
    "ds_read_b64_tr_b16 v[170:171], v221 offset:16640\n"
    "ds_read_b64_tr_b16 v[172:173], v221 offset:20480\n"
    "ds_read_b64_tr_b16 v[174:175], v221 offset:20736\n"
    "ds_read_b64_tr_b16 v[176:177], v221 offset:24576\n"
    "ds_read_b64_tr_b16 v[178:179], v221 offset:24832\n"
    "s_lshl_b32 s56, s61, 7\n"
    "v_add_u32 v221, s56, v194\n"
    "s_waitcnt lgkmcnt(12)\n"
    "v_mfma_f32_32x32x16_bf16 v[204:219], v[228:231], v[152:155], 0\n"
    "s_waitcnt lgkmcnt(10)\n"
    "v_mfma_f32_32x32x16_bf16 v[204:219], v[228:231], v[156:159], v[204:219]\n"
    "s_waitcnt lgkmcnt(8)\n"
    "v_mfma_f32_32x32x16_bf16 v[204:219], v[228:231], v[160:163], v[204:219]\n"
    "s_waitcnt lgkmcnt(6)\n"
    "v_mfma_f32_32x32x16_bf16 v[204:219], v[228:231], v[164:167], v[204:219]\n"
    "s_waitcnt lgkmcnt(4)\n"
    "v_mfma_f32_32x32x16_bf16 v[204:219], v[228:231], v[168:171], v[204:219]\n"
    "s_waitcnt lgkmcnt(2)\n"
    "v_mfma_f32_32x32x16_bf16 v[204:219], v[228:231], v[172:175], v[204:219]\n"
    "s_waitcnt lgkmcnt(0)\n"
    "v_mfma_f32_32x32x16_bf16 v[204:219], v[228:231], v[176:179], v[204:219]\n"
    "v_mfma_f32_32x32x16_bf16 v[136:151], v[124:127], v[44:47], v[136:151]\n"
    "ds_read_b64_tr_b16 v[120:121], v192 offset:12288\n"
    "ds_read_b64_tr_b16 v[122:123], v192 offset:12544\n"
    "v_mfma_f32_32x32x16_bf16 v[136:151], v[128:131], v[48:51], v[136:151]\n"
    "ds_read_b64_tr_b16 v[124:125], v192 offset:13312\n"
    "ds_read_b64_tr_b16 v[126:127], v192 offset:13568\n"
    "v_mfma_f32_32x32x16_bf16 v[136:151], v[132:135], v[52:55], v[136:151]\n"
    "ds_read_b64_tr_b16 v[128:129], v192 offset:14336\n"
    "ds_read_b64_tr_b16 v[130:131], v192 offset:14592\n"
    "s_waitcnt lgkmcnt(4)\n"
    "v_mfma_f32_32x32x16_bf16 v[136:151], v[120:123], v[56:59], v[136:151]\n"
    "ds_read_b64_tr_b16 v[132:133], v192 offset:15360\n"
    "ds_read_b64_tr_b16 v[134:135], v192 offset:15616\n"
    "ds_write_b32 v221, v204\n"
    "s_waitcnt lgkmcnt(5)\n"
    "v_mfma_f32_32x32x16_bf16 v[136:151], v[124:127], v[60:63], v[136:151]\n"
    "ds_read_b64_tr_b16 v[120:121], v192 offset:16384\n"
    "ds_read_b64_tr_b16 v[122:123], v192 offset:16640\n"
    "s_waitcnt lgkmcnt(5)\n"
    "v_mfma_f32_32x32x16_bf16 v[136:151], v[128:131], v[64:67], v[136:151]\n"
    "ds_read_b64_tr_b16 v[124:125], v192 offset:17408\n"
    "ds_read_b64_tr_b16 v[126:127], v192 offset:17664\n"
    "s_waitcnt lgkmcnt(5)\n"
    "v_mfma_f32_32x32x16_bf16 v[136:151], v[132:135], v[68:71], v[136:151]\n"
    "ds_read_b64_tr_b16 v[128:129], v192 offset:18432\n"
    "ds_read_b64_tr_b16 v[130:131], v192 offset:18688\n"
    "s_waitcnt lgkmcnt(4)\n"
    "v_mfma_f32_32x32x16_bf16 v[136:151], v[120:123], v[72:75], v[136:151]\n"
    "ds_read_b64_tr_b16 v[132:133], v192 offset:19456\n"
    "ds_read_b64_tr_b16 v[134:135], v192 offset:19712\n"
    "s_waitcnt lgkmcnt(4)\n"
    "v_mfma_f32_32x32x16_bf16 v[136:151], v[124:127], v[76:79], v[136:151]\n"
    "ds_read_b64_tr_b16 v[120:121], v192 offset:20480\n"
    "ds_read_b64_tr_b16 v[122:123], v192 offset:20736\n"
    "s_waitcnt lgkmcnt(4)\n"
    "v_mfma_f32_32x32x16_bf16 v[136:151], v[128:131], v[80:83], v[136:151]\n"
    "ds_read_b64_tr_b16 v[124:125], v192 offset:21504\n"
    "ds_read_b64_tr_b16 v[126:127], v192 offset:21760\n"
    "s_waitcnt lgkmcnt(4)\n"
    "v_mfma_f32_32x32x16_bf16 v[136:151], v[132:135], v[84:87], v[136:151]\n"
    "ds_read_b64_tr_b16 v[128:129], v192 offset:22528\n"
    "ds_read_b64_tr_b16 v[130:131], v192 offset:22784\n"
    "s_waitcnt lgkmcnt(4)\n"
    "v_mfma_f32_32x32x16_bf16 v[136:151], v[120:123], v[88:91], v[136:151]\n"
    "ds_read_b64_tr_b16 v[132:133], v192 offset:23552\n"
    "ds_read_b64_tr_b16 v[134:135], v192 offset:23808\n"
    "s_waitcnt lgkmcnt(4)\n"
    "v_mfma_f32_32x32x16_bf16 v[136:151], v[124:127], v[92:95], v[136:151]\n"
    "ds_read_b64_tr_b16 v[120:121], v192 offset:24576\n"
    "ds_read_b64_tr_b16 v[122:123], v192 offset:24832\n"
    "s_waitcnt lgkmcnt(4)\n"
    "v_mfma_f32_32x32x16_bf16 v[136:151], v[128:131], v[96:99], v[136:151]\n"
    "ds_read_b64_tr_b16 v[124:125], v192 offset:25600\n"
    "ds_read_b64_tr_b16 v[126:127], v192 offset:25856\n"
    "s_waitcnt lgkmcnt(4)\n"
    "v_mfma_f32_32x32x16_bf16 v[136:151], v[132:135], v[100:103], v[136:151]\n"
    "ds_read_b64_tr_b16 v[128:129], v192 offset:26624\n"
    "ds_read_b64_tr_b16 v[130:131], v192 offset:26880\n"
    "s_waitcnt lgkmcnt(4)\n"
    "v_mfma_f32_32x32x16_bf16 v[136:151], v[120:123], v[104:107], v[136:151]\n"
    "ds_read_b64_tr_b16 v[132:133], v192 offset:27648\n"
    "ds_read_b64_tr_b16 v[134:135], v192 offset:27904\n"
    "s_waitcnt lgkmcnt(4)\n"
    "v_mfma_f32_32x32x16_bf16 v[136:151], v[124:127], v[108:111], v[136:151]\n"
    "s_waitcnt lgkmcnt(2)\n"
    "v_mfma_f32_32x32x16_bf16 v[136:151], v[128:131], v[112:115], v[136:151]\n"
    "s_waitcnt lgkmcnt(0)\n"
    "v_mfma_f32_32x32x16_bf16 v[136:151], v[132:135], v[116:119], v[136:151]\n"
    "v_mov_b32 v211, v193\n"
    "s_barrier\n"
    "ds_read_b32 v152, v194\n"
    "ds_read_b32 v153, v194 offset:128\n"
    "ds_read_b32 v154, v194 offset:256\n"
    "ds_read_b32 v155, v194 offset:384\n"
    "s_cmp_lt_u32 s44, 7\n"
    "s_cselect_b32 s63, 1, 0\n"
    "s_cselect_b32 s44, s44, 0\n"
    "s_lshl_b32 s56, s44, 9\n"
    "s_mov_b32 s79, s40\n"
    "s_lshl_b32 s57, s79, 6\n"
    "s_add_u32 s74, s56, s57\n"
    "s_lshl_b32 s45, s40, 13\n"
    "s_add_u32 s45, s45, 0x18000\n"
    "v_lshrrev_b32 v2, 2, v196\n"
    "v_mul_u32_u24 v2, 0xe00, v2\n"
    "v_lshl_add_u32 v245, v200, 4, v2\n"
    "v_and_b32 v4, 1, v198\n"
    "v_lshlrev_b32 v2, 2, v4\n"
    "v_and_b32 v3, 7, v196\n"
    "v_xor_b32 v2, v2, v3\n"
    "v_lshrrev_b32 v3, 3, v196\n"
    "v_mul_u32_u24 v3, 0x380, v3\n"
    "v_lshl_add_u32 v244, v2, 4, v3\n"
    "v_lshl_add_u32 v5, v201, 2, v199\n"
    "v_lshlrev_b32 v3, 3, v200\n"
    "v_lshl_add_u32 v3, v4, 5, v3\n"
    "v_lshl_add_u32 v246, v5, 6, v3\n"
    "v_add_u32 v246, s45, v246\n"
    "v_lshlrev_b32 v247, 3, v197\n"
    "s_waitcnt lgkmcnt(2)\n"
    "v_add_f32 v152, v152, v153\n"
    "s_waitcnt lgkmcnt(0)\n"
    "v_add_f32 v154, v154, v155\n"
    "v_sub_f32 v152, -v152, v154\n"
    "v_cvt_pk_bf16_f32 v232, v152, v152\n"
    "v_lshlrev_b32 v153, 16, v232\n"
    "v_sub_f32 v152, v152, v153\n"
    "v_cvt_pk_bf16_f32 v233, v152, 0\n"
    "v_cndmask_b32 v232, 0, v232, s[50:51]\n"
    "v_cndmask_b32 v233, 0, v233, s[50:51]\n"
    "s_nop 1\n"
    "v_mfma_f32_32x32x16_bf16 v[136:151], v[232:235], v[236:239], v[136:151]\n"
    "v_lshlrev_b32 v2, 4, v197\n"
    "v_lshl_add_u32 v164, v198, 12, v2\n"
    "v_lshl_add_u32 v165, v198, 3, v2\n"
    "s_lshl_b32 s56, s40, 9\n"
    "s_add_u32 s57, s56, 0x15000\n"
    "v_add_u32 v165, s57, v165\n"
    "v_add_u32 v166, 0x15000, v1\n"
    "v_and_b32 v2, 1, v196\n"
    "v_lshlrev_b32 v2, 4, v2\n"
    "v_mov_b32 v3, 0x3f80\n"
    "v_lshlrev_b32 v2, v2, v3\n"
    "v_lshrrev_b32 v3, 1, v197\n"
    "v_cmp_eq_u32 vcc, 0, v3\n"
    "s_nop 1\n"
    "v_cndmask_b32 v160, 0, v2, vcc\n"
    "v_cmp_eq_u32 vcc, 1, v3\n"
    "s_nop 1\n"
    "v_cndmask_b32 v161, 0, v2, vcc\n"
    "v_cmp_eq_u32 vcc, 2, v3\n"
    "s_nop 1\n"
    "v_cndmask_b32 v162, 0, v2, vcc\n"
    "v_cmp_eq_u32 vcc, 3, v3\n"
    "s_nop 1\n"
    "v_cndmask_b32 v163, 0, v2, vcc\n"
    "s_lshl_b32 s59, s54, 16\n"
    "s_add_u32 s59, s59, s56\n"
    "v_cvt_pk_bf16_f32 v8, v136, v137\n"
    "v_cvt_pk_bf16_f32 v9, v138, v139\n"
    "v_cvt_pk_bf16_f32 v10, v140, v141\n"
    "v_cvt_pk_bf16_f32 v11, v142, v143\n"
    "v_cvt_pk_bf16_f32 v12, v144, v145\n"
    "v_cvt_pk_bf16_f32 v13, v146, v147\n"
    "v_cvt_pk_bf16_f32 v14, v148, v149\n"
    "v_cvt_pk_bf16_f32 v15, v150, v151\n"
    "v_mfma_f32_32x32x16_bf16 v[16:31], v[184:187], v[8:11], 0\n"
    "s_nop 0\n"
    "v_mfma_f32_32x32x16_bf16 v[16:31], v[188:191], v[12:15], v[16:31]\n"
    "s_nop 11\n"
    "v_max3_f32 v64, v16, v17, v18\n"
    "v_max3_f32 v65, -v16, -v17, -v18\n"
    "v_max3_f32 v64, v64, v19, v20\n"
    "v_max3_f32 v65, v65, -v19, -v20\n"
    "v_max3_f32 v64, v64, v21, v22\n"
    "v_max3_f32 v65, v65, -v21, -v22\n"
    "v_max3_f32 v64, v64, v23, v24\n"
    "v_max3_f32 v65, v65, -v23, -v24\n"
    "v_max3_f32 v64, v64, v25, v26\n"
    "v_max3_f32 v65, v65, -v25, -v26\n"
    "v_max3_f32 v64, v64, v27, v28\n"
    "v_max3_f32 v65, v65, -v27, -v28\n"
    "v_max_f32 v64, v64, v29\n"
    "v_max_f32 v65, v65, -v29\n"
    "s_nop 1\n"
    "v_permlane32_swap_b32 v64, v65\n"
    "v_max_f32 v64, v64, v65\n"
    "v_mul_f32 v64, v224, v64\n"
    "v_mov_b32 v65, v64\n"
    "s_nop 1\n"
    "v_permlane32_swap_b32 v64, v65\n"
    "v_pk_fma_f32 v[32:33], v[16:17], v[224:225], v[64:65] op_sel_hi:[1,0,0] neg_lo:[0,0,1] neg_hi:[0,0,1]\n"
    "v_pk_fma_f32 v[48:49], v[16:17], v[224:225], v[64:65] op_sel:[0,0,1] op_sel_hi:[1,0,1] neg_lo:[1,0,1] neg_hi:[1,0,1]\n"
    "v_pk_fma_f32 v[34:35], v[18:19], v[224:225], v[64:65] op_sel_hi:[1,0,0] neg_lo:[0,0,1] neg_hi:[0,0,1]\n"
    "v_pk_fma_f32 v[50:51], v[18:19], v[224:225], v[64:65] op_sel:[0,0,1] op_sel_hi:[1,0,1] neg_lo:[1,0,1] neg_hi:[1,0,1]\n"
    "v_pk_fma_f32 v[36:37], v[20:21], v[224:225], v[64:65] op_sel_hi:[1,0,0] neg_lo:[0,0,1] neg_hi:[0,0,1]\n"
    "v_pk_fma_f32 v[52:53], v[20:21], v[224:225], v[64:65] op_sel:[0,0,1] op_sel_hi:[1,0,1] neg_lo:[1,0,1] neg_hi:[1,0,1]\n"
    "v_pk_fma_f32 v[38:39], v[22:23], v[224:225], v[64:65] op_sel_hi:[1,0,0] neg_lo:[0,0,1] neg_hi:[0,0,1]\n"
    "v_pk_fma_f32 v[54:55], v[22:23], v[224:225], v[64:65] op_sel:[0,0,1] op_sel_hi:[1,0,1] neg_lo:[1,0,1] neg_hi:[1,0,1]\n"
    "v_pk_fma_f32 v[40:41], v[24:25], v[224:225], v[64:65] op_sel_hi:[1,0,0] neg_lo:[0,0,1] neg_hi:[0,0,1]\n"
    "v_pk_fma_f32 v[56:57], v[24:25], v[224:225], v[64:65] op_sel:[0,0,1] op_sel_hi:[1,0,1] neg_lo:[1,0,1] neg_hi:[1,0,1]\n"
    "v_pk_fma_f32 v[42:43], v[26:27], v[224:225], v[64:65] op_sel_hi:[1,0,0] neg_lo:[0,0,1] neg_hi:[0,0,1]\n"
    "v_pk_fma_f32 v[58:59], v[26:27], v[224:225], v[64:65] op_sel:[0,0,1] op_sel_hi:[1,0,1] neg_lo:[1,0,1] neg_hi:[1,0,1]\n"
    "v_pk_fma_f32 v[44:45], v[28:29], v[224:225], v[64:65] op_sel_hi:[1,0,0] neg_lo:[0,0,1] neg_hi:[0,0,1]\n"
    "v_pk_fma_f32 v[60:61], v[28:29], v[224:225], v[64:65] op_sel:[0,0,1] op_sel_hi:[1,0,1] neg_lo:[1,0,1] neg_hi:[1,0,1]\n"
    "v_exp_f32 v32, v32\n"
    "v_exp_f32 v48, v48\n"
    "v_exp_f32 v33, v33\n"
    "v_exp_f32 v49, v49\n"
    "v_exp_f32 v34, v34\n"
    "v_exp_f32 v50, v50\n"
    "v_exp_f32 v35, v35\n"
    "v_exp_f32 v51, v51\n"
    "v_pk_add_f32 v[64:65], v[32:33], v[34:35]\n"
    "v_pk_add_f32 v[66:67], v[48:49], v[50:51]\n"
    "v_exp_f32 v36, v36\n"
    "v_exp_f32 v52, v52\n"
    "v_exp_f32 v37, v37\n"
    "v_exp_f32 v53, v53\n"
    "v_pk_add_f32 v[64:65], v[64:65], v[36:37]\n"
    "v_pk_add_f32 v[66:67], v[66:67], v[52:53]\n"
    "v_exp_f32 v38, v38\n"
    "v_exp_f32 v54, v54\n"
    "v_exp_f32 v39, v39\n"
    "v_exp_f32 v55, v55\n"
    "v_pk_add_f32 v[64:65], v[64:65], v[38:39]\n"
    "v_pk_add_f32 v[66:67], v[66:67], v[54:55]\n"
    "v_exp_f32 v40, v40\n"
    "v_exp_f32 v56, v56\n"
    "v_exp_f32 v41, v41\n"
    "v_exp_f32 v57, v57\n"
    "v_pk_add_f32 v[64:65], v[64:65], v[40:41]\n"
    "v_pk_add_f32 v[66:67], v[66:67], v[56:57]\n"
    "v_exp_f32 v42, v42\n"
    "v_exp_f32 v58, v58\n"
    "v_exp_f32 v43, v43\n"
    "v_exp_f32 v59, v59\n"
    "v_pk_add_f32 v[64:65], v[64:65], v[42:43]\n"
    "v_pk_add_f32 v[66:67], v[66:67], v[58:59]\n"
    "v_exp_f32 v44, v44\n"
    "v_exp_f32 v60, v60\n"
    "v_exp_f32 v45, v45\n"
    "v_exp_f32 v61, v61\n"
    "v_pk_add_f32 v[64:65], v[64:65], v[44:45]\n"
    "v_pk_add_f32 v[66:67], v[66:67], v[60:61]\n"
    "v_add_f32 v64, v64, v65\n"
    "v_add_f32 v65, v66, v67\n"
    "s_nop 1\n"
    "v_permlane32_swap_b32 v64, v65\n"
    "v_add_f32 v64, v64, v65\n"
    "v_rcp_f32 v64, v64\n"
    "s_nop 0\n"
    "v_mov_b32 v65, v64\n"
    "v_mov_b32 v175, 0\n"
    "v_mov_b32 v183, 0\n"
    "v_permlane32_swap_b32 v64, v65\n"
    "v_pk_mul_f32 v[32:33], v[32:33], v[64:65] op_sel_hi:[1,0]\n"
    "v_pk_mul_f32 v[48:49], v[48:49], v[64:65] op_sel:[0,1] op_sel_hi:[1,1]\n"
    "v_pk_mul_f32 v[34:35], v[34:35], v[64:65] op_sel_hi:[1,0]\n"
    "v_pk_mul_f32 v[50:51], v[50:51], v[64:65] op_sel:[0,1] op_sel_hi:[1,1]\n"
    "v_pk_mul_f32 v[36:37], v[36:37], v[64:65] op_sel_hi:[1,0]\n"
    "v_pk_mul_f32 v[52:53], v[52:53], v[64:65] op_sel:[0,1] op_sel_hi:[1,1]\n"
    "v_pk_mul_f32 v[38:39], v[38:39], v[64:65] op_sel_hi:[1,0]\n"
    "v_pk_mul_f32 v[54:55], v[54:55], v[64:65] op_sel:[0,1] op_sel_hi:[1,1]\n"
    "v_pk_mul_f32 v[40:41], v[40:41], v[64:65] op_sel_hi:[1,0]\n"
    "v_pk_mul_f32 v[56:57], v[56:57], v[64:65] op_sel:[0,1] op_sel_hi:[1,1]\n"
    "v_pk_mul_f32 v[42:43], v[42:43], v[64:65] op_sel_hi:[1,0]\n"
    "v_pk_mul_f32 v[58:59], v[58:59], v[64:65] op_sel:[0,1] op_sel_hi:[1,1]\n"
    "v_pk_mul_f32 v[44:45], v[44:45], v[64:65] op_sel_hi:[1,0]\n"
    "v_pk_mul_f32 v[60:61], v[60:61], v[64:65] op_sel:[0,1] op_sel_hi:[1,1]\n"
    "v_cvt_pk_bf16_f32 v168, v32, v33\n"
    "v_cvt_pk_bf16_f32 v176, v48, v49\n"
    "v_cvt_pk_bf16_f32 v169, v34, v35\n"
    "v_cvt_pk_bf16_f32 v177, v50, v51\n"
    "v_cvt_pk_bf16_f32 v170, v36, v37\n"
    "v_cvt_pk_bf16_f32 v178, v52, v53\n"
    "v_cvt_pk_bf16_f32 v171, v38, v39\n"
    "v_cvt_pk_bf16_f32 v179, v54, v55\n"
    "v_cvt_pk_bf16_f32 v172, v40, v41\n"
    "v_cvt_pk_bf16_f32 v180, v56, v57\n"
    "v_cvt_pk_bf16_f32 v173, v42, v43\n"
    "v_cvt_pk_bf16_f32 v181, v58, v59\n"
    "v_cvt_pk_bf16_f32 v174, v44, v45\n"
    "v_cvt_pk_bf16_f32 v182, v60, v61\n"
    ".Lfb_rj5:\n"
    "s_barrier\n"
    "s_add_u32 s56, s59, 0x10000\n"
    "buffer_load_dwordx4 v[64:67], v164, s[12:15], s56 offen sc1\n"
    "buffer_load_dwordx4 v[68:71], v164, s[12:15], s56 offen offset:256 sc1\n"
    "s_add_u32 s56, s59, 0x14000\n"
    "buffer_load_dwordx4 v[72:75], v164, s[12:15], s56 offen sc1\n"
    "buffer_load_dwordx4 v[76:79], v164, s[12:15], s56 offen offset:256 sc1\n"
    "s_add_u32 s56, s59, 0x18000\n"
    "buffer_load_dwordx4 v[80:83], v164, s[12:15], s56 offen sc1\n"
    "buffer_load_dwordx4 v[84:87], v164, s[12:15], s56 offen offset:256 sc1\n"
    "s_add_u32 s56, s59, 0x1c000\n"
    "buffer_load_dwordx4 v[88:91], v164, s[12:15], s56 offen sc1\n"
    "buffer_load_dwordx4 v[92:95], v164, s[12:15], s56 offen offset:256 sc1\n"
    "s_mul_i32 s75, s40, 12288\n"
    "v_lshrrev_b32 v2, 1, v199\n"
    "s_lshr_b32 s56, s61, 1\n"
    "v_xor_b32 v2, s56, v2\n"
    "v_xor_b32 v3, s61, v198\n"
    "v_and_b32 v3, 1, v3\n"
    "v_lshlrev_b32 v2, 6, v2\n"
    "v_lshl_add_u32 v2, v3, 5, v2\n"
    "v_lshl_add_u32 v2, v200, 3, v2\n"
    "v_lshl_add_u32 v2, v5, 7, v2\n"
    "s_mul_i32 s56, s61, 14336\n"
    "v_add_u32 v184, s56, v2\n"
    "v_xor_b32 v186, 64, v184\n"
    "v_mov_b32 v3, 0x400\n"
    "v_cndmask_b32 v3, 0, v3, s[50:51]\n"
    "v_add_u32 v185, v3, v184\n"
    "v_add_u32 v187, v3, v186\n"
    "s_mul_i32 s57, s62, 24576\n"
    "s_xor_b32 s56, s61, 1\n"
    "s_mul_i32 s56, s56, 6144\n"
    "s_add_u32 s56, s56, s57\n"
    "v_add_u32 v188, s56, v1\n"
    "s_xor_b32 s56, s61, 2\n"
    "s_mul_i32 s56, s56, 6144\n"
    "s_add_u32 s56, s56, s57\n"
    "v_add_u32 v189, s56, v1\n"
    "s_xor_b32 s56, s61, 3\n"
    "s_mul_i32 s56, s56, 6144\n"
    "s_add_u32 s56, s56, s57\n"
    "v_add_u32 v190, s56, v1\n"
    "s_xor_b32 s56, s61, 0\n"
    "s_mul_i32 s56, s56, 6144\n"
    "s_add_u32 s56, s56, s57\n"
    "v_add_u32 v191, s56, v1\n"
    "v_bfe_u32 v2, v196, 1, 3\n"
    "s_lshl_b32 s56, s61, 1\n"
    "v_xor_b32 v2, s56, v2\n"
    "v_lshlrev_b32 v2, 4, v2\n"
    "v_lshl_add_u32 v2, v201, 3, v2\n"
    "v_lshl_add_u32 v2, v202, 7, v2\n"
    "s_lshl_b32 s56, s62, 12\n"
    "s_add_u32 s56, s56, 0x10000\n"
    "v_add_u32 v192, s56, v2\n"
    "s_mul_i32 s73, s43, 73728\n"
    "s_add_u32 s73, s73, 0x310000\n"
    "s_add_u32 s56, s44, 0\n"
    "s_sub_u32 s57, s56, 7\n"
    "s_cmp_ge_u32 s56, 7\n"
    "s_cselect_b32 s56, s57, s56\n"
    "s_lshl_b32 s66, s56, 14\n"
    "s_add_u32 s56, s44, 1\n"
    "s_sub_u32 s57, s56, 7\n"
    "s_cmp_ge_u32 s56, 7\n"
    "s_cselect_b32 s56, s57, s56\n"
    "s_lshl_b32 s67, s56, 14\n"
    "s_add_u32 s56, s44, 2\n"
    "s_sub_u32 s57, s56, 7\n"
    "s_cmp_ge_u32 s56, 7\n"
    "s_cselect_b32 s56, s57, s56\n"
    "s_lshl_b32 s68, s56, 14\n"
    "s_add_u32 s56, s44, 3\n"
    "s_sub_u32 s57, s56, 7\n"
    "s_cmp_ge_u32 s56, 7\n"
    "s_cselect_b32 s56, s57, s56\n"
    "s_lshl_b32 s69, s56, 14\n"
    "s_add_u32 s56, s44, 4\n"
    "s_sub_u32 s57, s56, 7\n"
    "s_cmp_ge_u32 s56, 7\n"
    "s_cselect_b32 s56, s57, s56\n"
    "s_lshl_b32 s70, s56, 14\n"
    "s_add_u32 s56, s44, 5\n"
    "s_sub_u32 s57, s56, 7\n"
    "s_cmp_ge_u32 s56, 7\n"
    "s_cselect_b32 s56, s57, s56\n"
    "s_lshl_b32 s71, s56, 14\n"
    "s_add_u32 s56, s44, 6\n"
    "s_sub_u32 s57, s56, 7\n"
    "s_cmp_ge_u32 s56, 7\n"
    "s_cselect_b32 s56, s57, s56\n"
    "s_lshl_b32 s72, s56, 14\n"
    "s_mul_i32 s56, s79, 28672\n"
    "s_lshl_b32 s57, s44, 7\n"
    "s_add_u32 s76, s56, s57\n"
    "v_lshrrev_b32 v2, 1, v199\n"
    "v_lshlrev_b32 v2, 6, v2\n"
    "v_and_b32 v3, 1, v198\n"
    "v_lshl_add_u32 v2, v3, 5, v2\n"
    "v_lshl_add_u32 v2, v200, 3, v2\n"
    "v_lshl_add_u32 v2, v5, 7, v2\n"
    "v_add_u32 v203, s75, v2\n"
    "v_xor_b32 v249, 64, v203\n"
    "v_and_b32 v3, 7, v196\n"
    "v_min_u32 v250, 6, v3\n"
    "v_lshlrev_b32 v250, 3, v250\n"
    "v_lshlrev_b32 v252, 3, v196\n"
    "s_sub_u32 s56, s75, s45\n"
    "v_add_u32 v253, s56, v246\n"
    "v_lshlrev_b32 v251, 4, v202\n"
    "v_add_u32 v251, 0x26000, v251\n"
    "v_add_u32 v248, 0x18000, v1\n"
    "s_lshl2_add_u32 s57, s41, s61\n"
    "s_lshl_b32 s57, s57, 15\n"
    "s_lshl_b32 s58, s54, 11\n"
    "s_add_u32 s57, s57, s58\n"
    "s_add_u32 s59, s57, 0x110000\n"
    "s_lshl_b32 s57, s41, 8\n"
    "s_lshl3_add_u32 s60, s54, s57\n"
    "s_waitcnt vmcnt(7)\n"
    "v_mfma_f32_16x16x32_bf16 v[96:99], v[160:163], v[64:67], 0\n"
    "s_waitcnt vmcnt(6)\n"
    "v_mfma_f32_16x16x32_bf16 v[100:103], v[160:163], v[68:71], 0\n"
    "s_waitcnt vmcnt(5)\n"
    "v_mfma_f32_16x16x32_bf16 v[96:99], v[160:163], v[72:75], v[96:99]\n"
    "s_waitcnt vmcnt(4)\n"
    "v_mfma_f32_16x16x32_bf16 v[100:103], v[160:163], v[76:79], v[100:103]\n"
    "s_waitcnt vmcnt(3)\n"
    "v_mfma_f32_16x16x32_bf16 v[96:99], v[160:163], v[80:83], v[96:99]\n"
    "s_waitcnt vmcnt(2)\n"
    "v_mfma_f32_16x16x32_bf16 v[100:103], v[160:163], v[84:87], v[100:103]\n"
    "s_waitcnt vmcnt(1)\n"
    "v_mfma_f32_16x16x32_bf16 v[96:99], v[160:163], v[88:91], v[96:99]\n"
    "s_waitcnt vmcnt(0)\n"
    "v_mfma_f32_16x16x32_bf16 v[100:103], v[160:163], v[92:95], v[100:103]\n"
    "s_nop 5\n"
    "v_cvt_pk_bf16_f32 v104, v96, v97\n"
    "v_cvt_pk_bf16_f32 v105, v98, v99\n"
    "v_cvt_pk_bf16_f32 v106, v100, v101\n"
    "v_cvt_pk_bf16_f32 v107, v102, v103\n"
    "s_mov_b64 exec, s[50:51]\n"
    "ds_write2_b64 v165, v[104:105], v[106:107] offset1:32\n"
    "s_mov_b64 exec, -1\n"
    "s_waitcnt lgkmcnt(0)\n"
    "s_barrier\n"
    "s_cmp_lt_u32 s40, 4\n"
    "s_cbranch_scc1 .Lfb_ok13\n"
    "ds_read_b128 v[64:67], v166\n"
    "ds_read_b128 v[68:71], v166 offset:1024\n"
    "ds_read_b128 v[72:75], v166 offset:2048\n"
    "ds_read_b128 v[76:79], v166 offset:3072\n"
    "s_waitcnt lgkmcnt(3)\n"
    "v_mfma_f32_32x32x16_bf16 v[136:151], v[64:67], v[168:171], 0\n"
    "s_waitcnt lgkmcnt(2)\n"
    "v_mfma_f32_32x32x16_bf16 v[136:151], v[68:71], v[172:175], v[136:151]\n"
    "s_waitcnt lgkmcnt(1)\n"
    "v_mfma_f32_32x32x16_bf16 v[136:151], v[72:75], v[176:179], v[136:151]\n"
    "s_waitcnt lgkmcnt(0)\n"
    "v_mfma_f32_32x32x16_bf16 v[136:151], v[76:79], v[180:183], v[136:151]\n"
    "s_nop 11\n"
    "v_mov_b32 v2, v148\n"
    "v_mov_b32 v3, v148\n"
    "s_nop 1\n"
    "v_permlane32_swap_b32 v2, v3\n"
    "v_add_f32 v2, 0x358637bd, v3\n"
    "v_rcp_f32 v2, v2\n"
    "s_nop 0\n"
    "v_pk_mul_f32 v[136:137], v[136:137], v[2:3] op_sel_hi:[1,0]\n"
    "v_pk_mul_f32 v[138:139], v[138:139], v[2:3] op_sel_hi:[1,0]\n"
    "v_pk_mul_f32 v[140:141], v[140:141], v[2:3] op_sel_hi:[1,0]\n"
    "v_pk_mul_f32 v[142:143], v[142:143], v[2:3] op_sel_hi:[1,0]\n"
    "v_pk_mul_f32 v[144:145], v[144:145], v[2:3] op_sel_hi:[1,0]\n"
    "v_pk_mul_f32 v[146:147], v[146:147], v[2:3] op_sel_hi:[1,0]\n"
    "v_pk_mul_f32 v[148:149], v[148:149], v[2:3] op_sel_hi:[1,0]\n"
    "v_pk_mul_f32 v[150:151], v[150:151], v[2:3] op_sel_hi:[1,0]\n"
    "v_cvt_pk_bf16_f32 v120, v136, v137\n"
    "v_cvt_pk_bf16_f32 v121, v138, v139\n"
    "v_cvt_pk_bf16_f32 v122, v140, v141\n"
    "v_cvt_pk_bf16_f32 v123, v142, v143\n"
    "buffer_store_dwordx4 v[120:123], v1, s[12:15], s59 offen sc1\n"
    "v_cvt_pk_bf16_f32 v124, v144, v145\n"
    "v_cvt_pk_bf16_f32 v125, v146, v147\n"
    "v_cvt_pk_bf16_f32 v126, v148, v149\n"
    "v_cvt_pk_bf16_f32 v127, v150, v151\n"
    "v_cndmask_b32 v126, v126, 0, s[52:53]\n"
    "v_cndmask_b32 v127, v127, 0, s[52:53]\n"
    "buffer_store_dwordx4 v[124:127], v1, s[12:15], s59 offen offset:1024 sc1\n"
    "ds_read_b64_tr_b16 v[8:9], v246\n"
    "ds_read_b64_tr_b16 v[10:11], v246 offset:512\n"
    "ds_read_b64_tr_b16 v[12:13], v246 offset:1024\n"
    "ds_read_b64_tr_b16 v[14:15], v246 offset:1536\n"
    "ds_read_b64_tr_b16 v[16:17], v246 offset:2048\n"
    "ds_read_b64_tr_b16 v[18:19], v246 offset:2560\n"
    "ds_read_b64_tr_b16 v[20:21], v246 offset:3072\n"
    "ds_read_b64_tr_b16 v[22:23], v246 offset:3584\n"
    "ds_read_b64_tr_b16 v[24:25], v246 offset:4096\n"
    "ds_read_b64_tr_b16 v[26:27], v246 offset:4608\n"
    "ds_read_b64_tr_b16 v[28:29], v246 offset:5120\n"
    "ds_read_b64_tr_b16 v[30:31], v246 offset:5632\n"
    "ds_read_b64_tr_b16 v[32:33], v246 offset:6144\n"
    "ds_read_b64_tr_b16 v[34:35], v246 offset:6656\n"
    "ds_read_b64_tr_b16 v[36:37], v246 offset:7168\n"
    "ds_read_b64_tr_b16 v[38:39], v246 offset:7680\n"
    "s_waitcnt lgkmcnt(0)\n"
    "s_mov_b32 m0, s45\n"
    "s_add_u32 s56, s74, 0x70000\n"
    "buffer_load_dwordx4 v245, s[20:23], s56 offen lds\n"
    "s_addk_i32 m0, 0x400\n"
    "s_add_u32 s56, s56, 0xe000\n"
    "buffer_load_dwordx4 v245, s[20:23], s56 offen lds\n"
    "s_waitcnt vmcnt(2)\n"
    "s_branch .Lfb_oj14\n"
    ".Lfb_ok13:\n"
    "ds_read_b64_tr_b16 v[8:9], v246\n"
    "ds_read_b64_tr_b16 v[10:11], v246 offset:512\n"
    "ds_read_b64_tr_b16 v[12:13], v246 offset:1024\n"
    "ds_read_b64_tr_b16 v[14:15], v246 offset:1536\n"
    "ds_read_b64_tr_b16 v[16:17], v246 offset:2048\n"
    "ds_read_b64_tr_b16 v[18:19], v246 offset:2560\n"
    "ds_read_b64_tr_b16 v[20:21], v246 offset:3072\n"
    "ds_read_b64_tr_b16 v[22:23], v246 offset:3584\n"
    "ds_read_b64_tr_b16 v[24:25], v246 offset:4096\n"
    "ds_read_b64_tr_b16 v[26:27], v246 offset:4608\n"
    "ds_read_b64_tr_b16 v[28:29], v246 offset:5120\n"
    "ds_read_b64_tr_b16 v[30:31], v246 offset:5632\n"
    "ds_read_b64_tr_b16 v[32:33], v246 offset:6144\n"
    "ds_read_b64_tr_b16 v[34:35], v246 offset:6656\n"
    "ds_read_b64_tr_b16 v[36:37], v246 offset:7168\n"
    "ds_read_b64_tr_b16 v[38:39], v246 offset:7680\n"
    "s_waitcnt lgkmcnt(0)\n"
    "s_mov_b32 m0, s45\n"
    "s_add_u32 s56, s74, 0x70000\n"
    "buffer_load_dwordx4 v245, s[20:23], s56 offen lds\n"
    "s_addk_i32 m0, 0x400\n"
    "s_add_u32 s56, s56, 0xe000\n"
    "buffer_load_dwordx4 v245, s[20:23], s56 offen lds\n"
    ".Lfb_oj14:\n"
    "s_barrier\n"
    "s_cmp_lg_u32 s40, 0\n"
    "s_cbranch_scc1 .Lfb_nf15\n"
    "s_add_u32 s56, s60, 0x2000\n"
    "s_mov_b64 exec, 1\n"
    "buffer_store_dwordx2 v[6:7], off, s[12:15], s56 sc1\n"
    "s_mov_b64 exec, -1\n"
    ".Lfb_nf15:\n"
    "s_cmp_eq_u32 s63, 0\n"
    "s_cbranch_scc1 .Lfb_idle\n"
    "s_and_b32 s56, s40, 1\n"
    "s_lshl_b32 s56, s56, 6\n"
    "v_xor_b32 v2, s56, v211\n"
    "s_lshl3_add_u32 s57, s43, s40\n"
    "s_mul_i32 s57, s57, 7168\n"
    "s_lshl_b32 s58, s44, 7\n"
    "s_add_u32 s57, s57, s58\n"
    "s_lshl_b32 s58, s40, 10\n"
    "s_add_u32 m0, s58, 0x10000\n"
    "s_mov_b32 s56, s57\n"
    "buffer_load_dwordx4 v2, s[4:7], s56 offen lds\n"
    "s_add_u32 m0, s45, 0x800\n"
    "s_add_u32 s56, s74, 0x8c000\n"
    "buffer_load_dwordx4 v245, s[20:23], s56 offen lds\n"
    "s_add_u32 m0, s45, 0xc00\n"
    "s_add_u32 s56, s74, 0x9a000\n"
    "buffer_load_dwordx4 v245, s[20:23], s56 offen lds\n"
    "s_barrier\n"
    "s_add_u32 m0, s45, 0x1000\n"
    "s_add_u32 s56, s74, 0xa8000\n"
    "buffer_load_dwordx4 v245, s[20:23], s56 offen lds\n"
    "s_add_u32 m0, s45, 0x1400\n"
    "s_add_u32 s56, s74, 0xb6000\n"
    "buffer_load_dwordx4 v245, s[20:23], s56 offen lds\n"
    "s_barrier\n"
    "s_add_u32 m0, s45, 0x1800\n"
    "s_add_u32 s56, s74, 0xc4000\n"
    "buffer_load_dwordx4 v245, s[20:23], s56 offen lds\n"
    "s_add_u32 m0, s45, 0x1c00\n"
    "s_add_u32 s56, s74, 0xd2000\n"
    "buffer_load_dwordx4 v245, s[20:23], s56 offen lds\n"
    "v_lshlrev_b32 v3, 1, v202\n"
    "buffer_load_ushort v195, v3, s[32:35], s74 offen\n"
    "ds_read_b64_tr_b16 v[120:121], v184\n"
    "ds_read_b64_tr_b16 v[122:123], v184 offset:1024\n"
    "ds_read_b64_tr_b16 v[128:129], v184 offset:2048\n"
    "ds_read_b64_tr_b16 v[130:131], v185 offset:2048\n"
    "ds_read_b64_tr_b16 v[124:125], v186\n"
    "ds_read_b64_tr_b16 v[126:127], v186 offset:1024\n"
    "ds_read_b64_tr_b16 v[132:133], v186 offset:2048\n"
    "ds_read_b64_tr_b16 v[134:135], v187 offset:2048\n"
    "ds_read_b64_tr_b16 v[136:137], v184 offset:3584\n"
    "ds_read_b64_tr_b16 v[138:139], v184 offset:4608\n"
    "ds_read_b64_tr_b16 v[144:145], v184 offset:5632\n"
    "ds_read_b64_tr_b16 v[146:147], v185 offset:5632\n"
    "ds_read_b64_tr_b16 v[140:141], v186 offset:3584\n"
    "ds_read_b64_tr_b16 v[142:143], v186 offset:4608\n"
    "ds_read_b64_tr_b16 v[148:149], v186 offset:5632\n"
    "ds_read_b64_tr_b16 v[150:151], v187 offset:5632\n"
    "ds_read_b64_tr_b16 v[152:153], v184 offset:7168\n"
    "ds_read_b64_tr_b16 v[154:155], v184 offset:8192\n"
    "ds_read_b64_tr_b16 v[160:161], v184 offset:9216\n"
    "ds_read_b64_tr_b16 v[162:163], v185 offset:9216\n"
    "ds_read_b64_tr_b16 v[156:157], v186 offset:7168\n"
    "ds_read_b64_tr_b16 v[158:159], v186 offset:8192\n"
    "ds_read_b64_tr_b16 v[164:165], v186 offset:9216\n"
    "ds_read_b64_tr_b16 v[166:167], v187 offset:9216\n"
    "ds_read_b64_tr_b16 v[168:169], v184 offset:10752\n"
    "ds_read_b64_tr_b16 v[170:171], v184 offset:11776\n"
    "ds_read_b64_tr_b16 v[176:177], v184 offset:12800\n"
    "ds_read_b64_tr_b16 v[178:179], v185 offset:12800\n"
    "ds_read_b64_tr_b16 v[172:173], v186 offset:10752\n"
    "ds_read_b64_tr_b16 v[174:175], v186 offset:11776\n"
    "ds_read_b64_tr_b16 v[180:181], v186 offset:12800\n"
    "ds_read_b64_tr_b16 v[182:183], v187 offset:12800\n"
    "s_lshl_b32 s59, s43, 16\n"
    "s_lshl_b32 s56, s40, 13\n"
    "s_add_u32 s59, s59, s56\n"
    "s_lshr_b32 s57, s43, 1\n"
    "s_lshl_b32 s57, s57, 8\n"
    "s_waitcnt vmcnt(7)\n"
    "s_cmp_lg_u32 s40, 0\n"
    "s_cbranch_scc1 .Lfb_np16\n"
    "s_add_u32 s56, s57, 0x2000\n"
    "s_mov_b32 s64, 0x30d40\n"
    ".Lfb_pl17:\n"
    "buffer_load_dwordx2 v[2:3], v247, s[12:15], s56 offen sc1\n"
    "s_waitcnt vmcnt(0)\n"
    "v_cmp_ne_u64 vcc, v[2:3], s[46:47]\n"
    "s_cbranch_vccz .Lfb_np16\n"
    "s_sleep 1\n"
    "s_sub_u32 s64, s64, 1\n"
    "s_cbranch_scc0 .Lfb_pl17\n"
    ".Lfb_np16:\n"
    "s_waitcnt lgkmcnt(0)\n"
    "s_barrier\n"
    "s_add_u32 s56, s59, 0x110000\n"
    "buffer_load_dwordx4 v[204:207], v1, s[12:15], s56 offen sc1\n"
    "buffer_load_dwordx4 v[208:211], v1, s[12:15], s56 offen offset:1024 sc1\n"
    "buffer_load_dwordx4 v[212:215], v1, s[12:15], s56 offen offset:2048 sc1\n"
    "buffer_load_dwordx4 v[216:219], v1, s[12:15], s56 offen offset:3072 sc1\n"
    "s_add_u32 s56, s59, 0x111000\n"
    "buffer_load_dwordx4 v[220:223], v1, s[12:15], s56 offen sc1\n"
    "buffer_load_dwordx4 v[224:227], v1, s[12:15], s56 offen offset:1024 sc1\n"
    "buffer_load_dwordx4 v[228:231], v1, s[12:15], s56 offen offset:2048 sc1\n"
    "buffer_load_dwordx4 v[232:235], v1, s[12:15], s56 offen offset:3072 sc1\n"
    "s_waitcnt vmcnt(9)\n"
    "ds_read_b64_tr_b16 v[40:41], v246\n"
    "ds_read_b64_tr_b16 v[42:43], v246 offset:512\n"
    "ds_read_b64_tr_b16 v[44:45], v246 offset:1024\n"
    "ds_read_b64_tr_b16 v[46:47], v246 offset:1536\n"
    "ds_read_b64_tr_b16 v[48:49], v246 offset:2048\n"
    "ds_read_b64_tr_b16 v[50:51], v246 offset:2560\n"
    "ds_read_b64_tr_b16 v[52:53], v246 offset:3072\n"
    "ds_read_b64_tr_b16 v[54:55], v246 offset:3584\n"
    "ds_read_b64_tr_b16 v[56:57], v246 offset:4096\n"
    "ds_read_b64_tr_b16 v[58:59], v246 offset:4608\n"
    "ds_read_b64_tr_b16 v[60:61], v246 offset:5120\n"
    "ds_read_b64_tr_b16 v[62:63], v246 offset:5632\n"
    "ds_read_b64_tr_b16 v[64:65], v246 offset:6144\n"
    "ds_read_b64_tr_b16 v[66:67], v246 offset:6656\n"
    "ds_read_b64_tr_b16 v[68:69], v246 offset:7168\n"
    "ds_read_b64_tr_b16 v[70:71], v246 offset:7680\n"
    "v_xor_b32 v4, 16, v192\n"
    "ds_read_b64 v[240:241], v192\n"
    "ds_read_b64 v[242:243], v4\n"
    "s_mul_i32 s57, s62, 28\n"
    "s_lshl2_add_u32 s58, s44, s61\n"
    "s_add_u32 s57, s57, s58\n"
    "s_lshl_b32 s57, s57, 10\n"
    "s_add_u32 s59, s57, s73\n"
    "s_lshl_b32 s58, s58, 6\n"
    "s_mul_i32 s56, s62, 7168\n"
    "s_add_u32 s58, s58, s56\n"
    "s_add_u32 s58, s58, s73\n"
    "s_add_u32 s60, s58, 0xe000\n"
    "v_lshrrev_b32 v2, 3, v202\n"
    "v_mul_u32_u24 v2, 0x700, v2\n"
    "v_and_b32 v3, 7, v196\n"
    "v_lshl_add_u32 v2, v3, 3, v2\n"
    "v_lshl_add_u32 v2, v201, 2, v2\n"
    "s_waitcnt vmcnt(7)\n"
    "v_mfma_f32_32x32x16_bf16 v[88:103], v[120:123], v[204:207], 0\n"
    "v_mfma_f32_32x32x16_bf16 v[104:119], v[124:127], v[204:207], 0\n"
    "s_waitcnt vmcnt(6)\n"
    "v_mfma_f32_32x32x16_bf16 v[88:103], v[128:131], v[208:211], v[88:103]\n"
    "v_mfma_f32_32x32x16_bf16 v[104:119], v[132:135], v[208:211], v[104:119]\n"
    "s_waitcnt vmcnt(5)\n"
    "v_mfma_f32_32x32x16_bf16 v[88:103], v[136:139], v[212:215], v[88:103]\n"
    "v_mfma_f32_32x32x16_bf16 v[104:119], v[140:143], v[212:215], v[104:119]\n"
    "s_waitcnt vmcnt(4)\n"
    "v_mfma_f32_32x32x16_bf16 v[88:103], v[144:147], v[216:219], v[88:103]\n"
    "v_mfma_f32_32x32x16_bf16 v[104:119], v[148:151], v[216:219], v[104:119]\n"
    "s_waitcnt vmcnt(3)\n"
    "v_mfma_f32_32x32x16_bf16 v[88:103], v[152:155], v[220:223], v[88:103]\n"
    "v_mfma_f32_32x32x16_bf16 v[104:119], v[156:159], v[220:223], v[104:119]\n"
    "s_waitcnt vmcnt(2)\n"
    "v_mfma_f32_32x32x16_bf16 v[88:103], v[160:163], v[224:227], v[88:103]\n"
    "v_mfma_f32_32x32x16_bf16 v[104:119], v[164:167], v[224:227], v[104:119]\n"
    "s_waitcnt vmcnt(1)\n"
    "v_mfma_f32_32x32x16_bf16 v[88:103], v[168:171], v[228:231], v[88:103]\n"
    "v_mfma_f32_32x32x16_bf16 v[104:119], v[172:175], v[228:231], v[104:119]\n"
    "s_waitcnt vmcnt(0)\n"
    "v_mfma_f32_32x32x16_bf16 v[88:103], v[176:179], v[232:235], v[88:103]\n"
    "v_mfma_f32_32x32x16_bf16 v[104:119], v[180:183], v[232:235], v[104:119]\n"
    "s_waitcnt lgkmcnt(1)\n"
    "v_lshlrev_b32 v204, 16, v240\n"
    "v_and_b32 v205, 0xffff0000, v240\n"
    "v_lshlrev_b32 v206, 16, v241\n"
    "v_and_b32 v207, 0xffff0000, v241\n"
    "s_waitcnt lgkmcnt(0)\n"
    "v_lshlrev_b32 v208, 16, v242\n"
    "v_and_b32 v209, 0xffff0000, v242\n"
    "v_lshlrev_b32 v210, 16, v243\n"
    "v_and_b32 v211, 0xffff0000, v243\n"
    "s_nop 0\n"
    "ds_write_b128 v188, v[96:99]\n"
    "ds_write_b128 v188, v[100:103] offset:1024\n"
    "ds_write_b128 v189, v[104:107] offset:2048\n"
    "ds_write_b128 v189, v[108:111] offset:3072\n"
    "ds_write_b128 v190, v[112:115] offset:4096\n"
    "ds_write_b128 v190, v[116:119] offset:5120\n"
    "v_pk_add_f32 v[204:205], v[204:205], v[88:89]\n"
    "v_pk_add_f32 v[206:207], v[206:207], v[90:91]\n"
    "v_pk_add_f32 v[208:209], v[208:209], v[92:93]\n"
    "v_pk_add_f32 v[210:211], v[210:211], v[94:95]\n"
    "s_waitcnt lgkmcnt(0)\n"
    "s_barrier\n"
    "ds_read_b128 v[212:215], v191\n"
    "ds_read_b128 v[216:219], v191 offset:1024\n"
    "ds_read_b128 v[220:223], v191 offset:2048\n"
    "ds_read_b128 v[224:227], v191 offset:3072\n"
    "ds_read_b128 v[228:231], v191 offset:4096\n"
    "ds_read_b128 v[232:235], v191 offset:5120\n"
    "s_waitcnt lgkmcnt(5)\n"
    "v_pk_add_f32 v[204:205], v[204:205], v[212:213]\n"
    "v_pk_add_f32 v[206:207], v[206:207], v[214:215]\n"
    "s_waitcnt lgkmcnt(4)\n"
    "v_pk_add_f32 v[208:209], v[208:209], v[216:217]\n"
    "v_pk_add_f32 v[210:211], v[210:211], v[218:219]\n"
    "s_waitcnt lgkmcnt(3)\n"
    "v_pk_add_f32 v[204:205], v[204:205], v[220:221]\n"
    "v_pk_add_f32 v[206:207], v[206:207], v[222:223]\n"
    "s_waitcnt lgkmcnt(2)\n"
    "v_pk_add_f32 v[208:209], v[208:209], v[224:225]\n"
    "v_pk_add_f32 v[210:211], v[210:211], v[226:227]\n"
    "s_waitcnt lgkmcnt(1)\n"
    "v_pk_add_f32 v[204:205], v[204:205], v[228:229]\n"
    "v_pk_add_f32 v[206:207], v[206:207], v[230:231]\n"
    "s_waitcnt lgkmcnt(0)\n"
    "v_pk_add_f32 v[208:209], v[208:209], v[232:233]\n"
    "v_pk_add_f32 v[210:211], v[210:211], v[234:235]\n"
    "v_cvt_pk_bf16_f32 v236, v204, v205\n"
    "v_cvt_pk_bf16_f32 v237, v206, v207\n"
    "v_cvt_pk_bf16_f32 v238, v208, v209\n"
    "v_cvt_pk_bf16_f32 v239, v210, v211\n"
    "buffer_store_dwordx4 v[236:239], v1, s[12:15], s59 offen sc1\n"
    "v_pk_add_f32 v[212:213], v[204:205], v[206:207]\n"
    "v_pk_mul_f32 v[214:215], v[204:205], v[204:205]\n"
    "v_pk_add_f32 v[212:213], v[212:213], v[208:209]\n"
    "v_pk_fma_f32 v[214:215], v[206:207], v[206:207], v[214:215]\n"
    "v_pk_add_f32 v[212:213], v[212:213], v[210:211]\n"
    "v_pk_fma_f32 v[214:215], v[208:209], v[208:209], v[214:215]\n"
    "v_add_f32 v212, v212, v213\n"
    "v_pk_fma_f32 v[214:215], v[210:211], v[210:211], v[214:215]\n"
    "s_nop 0\n"
    "v_add_f32 v213, v214, v215\n"
    "s_nop 1\n"
    "v_permlane32_swap_b32 v212, v213\n"
    "v_add_f32 v212, v212, v213\n"
    "buffer_store_dword v212, v2, s[12:15], s60 offen sc1\n"
    "s_mov_b32 m0, s45\n"
    "s_add_u32 s56, s74, 0xe0000\n"
    "buffer_load_dwordx4 v245, s[20:23], s56 offen lds\n"
    "s_add_u32 m0, s45, 0x400\n"
    "s_add_u32 s56, s74, 0xee000\n"
    "buffer_load_dwordx4 v245, s[20:23], s56 offen lds\n"
    "s_add_u32 m0, s45, 0x800\n"
    "s_add_u32 s56, s74, 0xfc000\n"
    "buffer_load_dwordx4 v245, s[20:23], s56 offen lds\n"
    "s_add_u32 m0, s45, 0xc00\n"
    "s_add_u32 s56, s74, 0x10a000\n"
    "buffer_load_dwordx4 v245, s[20:23], s56 offen lds\n"
    "s_lshl_b32 s57, s43, 8\n"
    "s_lshl3_add_u32 s58, s44, s57\n"
    "s_waitcnt vmcnt(4)\n"
    "s_barrier\n"
    "s_cmp_lg_u32 s40, 0\n"
    "s_cbranch_scc1 .Lfb_nf18\n"
    "s_add_u32 s56, s58, 0x3000\n"
    "s_mov_b64 exec, 1\n"
    "buffer_store_dwordx2 v[6:7], off, s[12:15], s56 sc1\n"
    "s_mov_b64 exec, -1\n"
    ".Lfb_nf18:\n"
    "s_add_u32 m0, s45, 0x1000\n"
    "s_add_u32 s56, s74, 0x118000\n"
    "buffer_load_dwordx4 v245, s[20:23], s56 offen lds\n"
    "s_add_u32 m0, s45, 0x1400\n"
    "s_add_u32 s56, s74, 0x126000\n"
    "buffer_load_dwordx4 v245, s[20:23], s56 offen lds\n"
    "s_barrier\n"
    "s_add_u32 m0, s45, 0x1800\n"
    "s_add_u32 s56, s74, 0x134000\n"
    "buffer_load_dwordx4 v245, s[20:23], s56 offen lds\n"
    "s_add_u32 m0, s45, 0x1c00\n"
    "s_add_u32 s56, s74, 0x142000\n"
    "buffer_load_dwordx4 v245, s[20:23], s56 offen lds\n"
    "s_barrier\n"
    "s_add_u32 m0, s75, 0x2000\n"
    "s_add_u32 s56, s74, 0x150000\n"
    "buffer_load_dwordx4 v245, s[20:23], s56 offen lds\n"
    "s_add_u32 m0, s75, 0x2400\n"
    "s_add_u32 s56, s74, 0x15e000\n"
    "buffer_load_dwordx4 v245, s[20:23], s56 offen lds\n"
    "s_barrier\n"
    "s_add_u32 m0, s75, 0x2800\n"
    "s_add_u32 s56, s74, 0x16c000\n"
    "buffer_load_dwordx4 v245, s[20:23], s56 offen lds\n"
    "s_add_u32 m0, s75, 0x2c00\n"
    "s_add_u32 s56, s74, 0x17a000\n"
    "buffer_load_dwordx4 v245, s[20:23], s56 offen lds\n"
    "s_waitcnt vmcnt(4)\n"
    "ds_read_b64_tr_b16 v[72:73], v246\n"
    "ds_read_b64_tr_b16 v[74:75], v246 offset:512\n"
    "ds_read_b64_tr_b16 v[76:77], v246 offset:1024\n"
    "ds_read_b64_tr_b16 v[78:79], v246 offset:1536\n"
    "ds_read_b64_tr_b16 v[80:81], v246 offset:2048\n"
    "ds_read_b64_tr_b16 v[82:83], v246 offset:2560\n"
    "ds_read_b64_tr_b16 v[84:85], v246 offset:3072\n"
    "ds_read_b64_tr_b16 v[86:87], v246 offset:3584\n"
    "ds_read_b64_tr_b16 v[88:89], v246 offset:4096\n"
    "ds_read_b64_tr_b16 v[90:91], v246 offset:4608\n"
    "ds_read_b64_tr_b16 v[92:93], v246 offset:5120\n"
    "ds_read_b64_tr_b16 v[94:95], v246 offset:5632\n"
    "ds_read_b64_tr_b16 v[96:97], v246 offset:6144\n"
    "ds_read_b64_tr_b16 v[98:99], v246 offset:6656\n"
    "ds_read_b64_tr_b16 v[100:101], v246 offset:7168\n"
    "ds_read_b64_tr_b16 v[102:103], v246 offset:7680\n"
    "s_mul_i32 s56, s40, 1792\n"
    "s_add_u32 s59, s56, s73\n"
    "s_add_u32 s59, s59, 0xe000\n"
    "s_lshl_b32 s57, s43, 8\n"
    "v_lshlrev_b32 v247, 3, v202\n"
    "s_mov_b32 s58, 0\n"
    "s_cmp_lg_u32 s40, 1\n"
    "s_cbranch_scc1 .Lfb_np19\n"
    "s_add_u32 s56, s58, 0x9000\n"
    "s_mov_b32 s64, 0x30d40\n"
    ".Lfb_pl20:\n"
    "buffer_load_dwordx2 v[2:3], v247, s[12:15], s56 offen sc1\n"
    "s_waitcnt vmcnt(0)\n"
    "v_cmp_ne_u64 vcc, v[2:3], s[46:47]\n"
    "s_cbranch_vccz .Lfb_np19\n"
    "s_sleep 1\n"
    "s_sub_u32 s64, s64, 1\n"
    "s_cbranch_scc0 .Lfb_pl20\n"
    ".Lfb_np19:\n"
    "s_cmp_lg_u32 s40, 0\n"
    "s_cbranch_scc1 .Lfb_np21\n"
    "s_add_u32 s56, s57, 0x3000\n"
    "s_mov_b32 s64, 0x30d40\n"
    ".Lfb_pl22:\n"
    "buffer_load_dwordx2 v[2:3], v250, s[12:15], s56 offen sc1\n"
    "s_waitcnt vmcnt(0)\n"
    "v_cmp_ne_u64 vcc, v[2:3], s[46:47]\n"
    "s_cbranch_vccz .Lfb_np21\n"
    "s_sleep 1\n"
    "s_sub_u32 s64, s64, 1\n"
    "s_cbranch_scc0 .Lfb_pl22\n"
    ".Lfb_np21:\n"
    "s_waitcnt lgkmcnt(0)\n"
    "s_barrier\n"
    "buffer_load_dwordx2 v[204:205], v252, s[12:15], s59 offen sc1\n"
    "buffer_load_dwordx2 v[210:211], v252, s[12:15], s59 offen offset:1536 sc1\n"
    "buffer_load_dwordx2 v[206:207], v252, s[12:15], s59 offen offset:512 sc1\n"
    "buffer_load_dwordx2 v[208:209], v252, s[12:15], s59 offen offset:1024 sc1\n"
    "s_lshl_b32 s57, s40, 10\n"
    "s_add_u32 s58, s57, s73\n"
    "s_add_u32 m0, s57, 0x18000\n"
    "s_mov_b32 s56, s58\n"
    "buffer_load_dwordx4 v1, s[12:15], s56 offen sc1 lds\n"
    "s_add_u32 m0, s57, 0x1a000\n"
    "s_add_u32 s56, s58, 0x2000\n"
    "buffer_load_dwordx4 v1, s[12:15], s56 offen sc1 lds\n"
    "s_add_u32 m0, s57, 0x1c000\n"
    "s_add_u32 s56, s58, 0x4000\n"
    "buffer_load_dwordx4 v1, s[12:15], s56 offen sc1 lds\n"
    "s_add_u32 m0, s57, 0x1e000\n"
    "s_add_u32 s56, s58, 0x6000\n"
    "buffer_load_dwordx4 v1, s[12:15], s56 offen sc1 lds\n"
    "s_barrier\n"
    "s_add_u32 m0, s57, 0x20000\n"
    "s_add_u32 s56, s58, 0x8000\n"
    "buffer_load_dwordx4 v1, s[12:15], s56 offen sc1 lds\n"
    "s_add_u32 m0, s57, 0x22000\n"
    "s_add_u32 s56, s58, 0xa000\n"
    "buffer_load_dwordx4 v1, s[12:15], s56 offen sc1 lds\n"
    "s_add_u32 m0, s57, 0x24000\n"
    "s_add_u32 s56, s58, 0xc000\n"
    "buffer_load_dwordx4 v1, s[12:15], s56 offen sc1 lds\n"
    "v_lshlrev_b32 v2, 2, v202\n"
    "s_lshl_b32 s56, s74, 1\n"
    "s_add_u32 s56, s56, 0xa000\n"
    "buffer_load_dword v193, v2, s[12:15], s56 offen sc1\n"
    "s_waitcnt vmcnt(12)\n"
    "ds_read_b64_tr_b16 v[104:105], v253 offset:8192\n"
    "ds_read_b64_tr_b16 v[106:107], v253 offset:8704\n"
    "ds_read_b64_tr_b16 v[108:109], v253 offset:9216\n"
    "ds_read_b64_tr_b16 v[110:111], v253 offset:9728\n"
    "ds_read_b64_tr_b16 v[112:113], v253 offset:10240\n"
    "ds_read_b64_tr_b16 v[114:115], v253 offset:10752\n"
    "ds_read_b64_tr_b16 v[116:117], v253 offset:11264\n"
    "ds_read_b64_tr_b16 v[118:119], v253 offset:11776\n"
    "s_waitcnt vmcnt(10)\n"
    "v_cndmask_b32 v210, 0, v210, s[50:51]\n"
    "v_cndmask_b32 v211, 0, v211, s[50:51]\n"
    "v_pk_add_f32 v[204:205], v[204:205], v[210:211]\n"
    "s_waitcnt vmcnt(8)\n"
    "v_pk_add_f32 v[206:207], v[206:207], v[208:209]\n"
    "s_nop 0\n"
    "v_pk_add_f32 v[204:205], v[204:205], v[206:207]\n"
    "s_nop 1\n"
    "v_permlane32_swap_b32 v204, v205\n"
    "v_add_f32 v204, v204, v205\n"
    "s_nop 1\n"
    "v_add_f32_dpp v204, v204, v204 row_ror:8 row_mask:0xf bank_mask:0xf\n"
    "v_mov_b32 v205, v204\n"
    "s_nop 1\n"
    "v_permlane16_swap_b32 v204, v205\n"
    "v_add_f32 v204, v204, v205\n"
    "v_mul_f32 v204, 0x3b124925, v204\n"
    "v_mov_b32 v205, v204\n"
    "s_nop 1\n"
    "v_permlane32_swap_b32 v204, v205\n"
    "v_fma_f32 v205, -v204, v204, v205\n"
    "v_max_f32 v205, 0, v205\n"
    "v_add_f32 v205, 0x3727c5ac, v205\n"
    "v_rsq_f32 v211, v205\n"
    "v_cvt_pk_bf16_f32 v206, v204, v204\n"
    "v_mul_f32 v205, v205, v211\n"
    "v_mul_f32 v211, 0x3fb8aa3b, v211\n"
    "v_cvt_pk_bf16_f32 v209, v204, v205\n"
    "v_lshlrev_b32 v206, 16, v209\n"
    "v_and_b32 v207, 0xffff0000, v209\n"
    "v_pk_add_f32 v[204:205], v[204:205], v[206:207] neg_lo:[0,1] neg_hi:[0,1]\n"
    "s_nop 0\n"
    "v_cvt_pk_bf16_f32 v204, v204, v205\n"
    "v_and_b32 v208, 0xffff, v209\n"
    "v_lshl_or_b32 v208, v204, 16, v208\n"
    "v_lshrrev_b32 v210, 16, v204\n"
    "s_lshl_b32 s56, s40, 7\n"
    "s_add_u32 s56, s56, 0x26000\n"
    "v_add_u32 v2, s56, v1\n"
    "s_mov_b64 exec, 0xff\n"
    "ds_write_b128 v2, v[208:211]\n"
    "s_mov_b64 exec, -1\n"
    "v_mov_b32 v255, 0x26400\n"
    "v_mov_b32 v2, 0\n"
    "v_mov_b32 v3, 0\n"
    "ds_write2_b64 v255, v[2:3], v[2:3] offset1:1\n"
    "s_lshl_b32 s54, s43, 9\n"
    "s_lshl3_add_u32 s55, s44, s54\n"
    "s_waitcnt vmcnt(4)\n"
    "s_waitcnt lgkmcnt(0)\n"
    "s_barrier\n"
    "ds_read_b128 v[156:159], v248\n"
    "ds_read_b128 v[160:163], v248 offset:1024\n"
    "ds_read_b128 v[164:167], v248 offset:2048\n"
    "ds_read_b128 v[172:175], v251\n"
    "ds_read_b128 v[176:179], v251 offset:512\n"
    "s_setprio 1\n"
    "s_cmp_gt_u32 s40, 3\n"
    "s_cbranch_scc1 .Lfb_pr23\n"
    "s_setprio 3\n"
    ".Lfb_pr23:\n"
    "s_cmp_gt_u32 s40, 3\n"
    "s_cbranch_scc0 .Lfb_gr24\n"
    "s_waitcnt vmcnt(1)\n"
    "s_barrier\n"
    ".Lfb_gr24:\n"
    "ds_read_b128 v[168:171], v248 offset:3072\n"
    "s_waitcnt lgkmcnt(5)\n"
    "v_mfma_f32_32x32x16_bf16 v[204:219], v[8:11], v[156:159], 0\n"
    "s_mul_i32 s57, s66, 14\n"
    "s_add_u32 m0, s75, 0x0\n"
    "s_add_u32 s57, s57, s76\n"
    "buffer_load_dwordx4 v244, s[24:27], s57 offen lds\n"
    "ds_read_b128 v[156:159], v248 offset:4096\n"
    "s_waitcnt lgkmcnt(5)\n"
    "v_mfma_f32_32x32x16_bf16 v[204:219], v[12:15], v[160:163], v[204:219]\n"
    "ds_read_b128 v[160:163], v248 offset:5120\n"
    "s_waitcnt lgkmcnt(5)\n"
    "v_mfma_f32_32x32x16_bf16 v[204:219], v[16:19], v[164:167], v[204:219]\n"
    "s_mul_i32 s57, s66, 14\n"
    "s_add_u32 m0, s75, 0x400\n"
    "s_add_u32 s57, s57, s76\n"
    "s_add_u32 s57, s57, 0x1c00\n"
    "buffer_load_dwordx4 v244, s[24:27], s57 offen lds\n"
    "ds_read_b128 v[164:167], v248 offset:6144\n"
    "s_waitcnt lgkmcnt(3)\n"
    "v_mfma_f32_32x32x16_bf16 v[204:219], v[20:23], v[168:171], v[204:219]\n"
    "ds_read_b128 v[168:171], v248 offset:7168\n"
    "s_waitcnt lgkmcnt(3)\n"
    "v_mfma_f32_32x32x16_bf16 v[204:219], v[24:27], v[156:159], v[204:219]\n"
    "s_mul_i32 s57, s66, 14\n"
    "s_add_u32 m0, s75, 0x800\n"
    "s_add_u32 s57, s57, s76\n"
    "s_add_u32 s57, s57, 0x3800\n"
    "buffer_load_dwordx4 v244, s[24:27], s57 offen lds\n"
    "s_mul_i32 s58, s43, 229376\n"
    "s_lshl_b32 s64, s40, 11\n"
    "s_add_u32 s58, s58, s64\n"
    "s_add_u32 s73, s58, 0x550000\n"
    "ds_read_b128 v[156:159], v248 offset:8192\n"
    "s_waitcnt lgkmcnt(3)\n"
    "v_mfma_f32_32x32x16_bf16 v[204:219], v[28:31], v[160:163], v[204:219]\n"
    "s_add_u32 s59, s73, s66\n"
    "v_mov_b32 v240, 0xbfb17218\n"
    "v_mov_b32 v241, 0x3f317218\n"
    "v_xor_b32 v2, v201, v197\n"
    "ds_read_b128 v[160:163], v248 offset:9216\n"
    "s_waitcnt lgkmcnt(3)\n"
    "v_mfma_f32_32x32x16_bf16 v[204:219], v[32:35], v[164:167], v[204:219]\n"
    "s_mul_i32 s57, s66, 14\n"
    "s_add_u32 m0, s75, 0xc00\n"
    "s_add_u32 s57, s57, s76\n"
    "s_add_u32 s57, s57, 0x5400\n"
    "buffer_load_dwordx4 v244, s[24:27], s57 offen lds\n"
    "v_lshlrev_b32 v2, 3, v2\n"
    "v_lshl_add_u32 v3, v202, 7, v2\n"
    "s_lshl_b32 s64, s40, 12\n"
    "s_add_u32 s64, s64, 0x18000\n"
    "ds_read_b128 v[164:167], v248 offset:10240\n"
    "s_waitcnt lgkmcnt(3)\n"
    "v_mfma_f32_32x32x16_bf16 v[204:219], v[36:39], v[168:171], v[204:219]\n"
    "v_add_u32 v254, s64, v3\n"
    "s_lshl_b32 s64, s61, 5\n"
    "v_xor_b32 v2, s64, v2\n"
    "v_and_b32 v3, 7, v196\n"
    "ds_read_b128 v[168:171], v248 offset:11264\n"
    "s_waitcnt lgkmcnt(3)\n"
    "v_mfma_f32_32x32x16_bf16 v[204:219], v[40:43], v[156:159], v[204:219]\n"
    "s_mul_i32 s57, s67, 14\n"
    "s_add_u32 m0, s75, 0x1000\n"
    "s_add_u32 s57, s57, s76\n"
    "buffer_load_dwordx4 v244, s[24:27], s57 offen lds\n"
    "v_lshl_add_u32 v2, v3, 7, v2\n"
    "v_lshrrev_b32 v3, 3, v202\n"
    "v_mul_u32_u24 v3, 0x3000, v3\n"
    "s_mul_i32 s64, s62, 49152\n"
    "ds_read_b128 v[156:159], v248 offset:12288\n"
    "s_waitcnt lgkmcnt(3)\n"
    "v_mfma_f32_32x32x16_bf16 v[204:219], v[44:47], v[160:163], v[204:219]\n"
    "s_addk_i32 s64, 0x1000\n"
    "v_add3_u32 v253, v2, v3, s64\n"
    "s_lshl_b32 s64, s40, 2\n"
    "v_add_u32 v2, s64, v198\n"
    "ds_read_b128 v[160:163], v248 offset:13312\n"
    "s_waitcnt lgkmcnt(3)\n"
    "v_mfma_f32_32x32x16_bf16 v[204:219], v[48:51], v[164:167], v[204:219]\n"
    "s_mul_i32 s57, s67, 14\n"
    "s_add_u32 m0, s75, 0x1400\n"
    "s_add_u32 s57, s57, s76\n"
    "s_add_u32 s57, s57, 0x1c00\n"
    "buffer_load_dwordx4 v244, s[24:27], s57 offen lds\n"
    "v_and_b32 v3, 15, v2\n"
    "v_xor_b32 v3, v3, v197\n"
    "v_lshlrev_b32 v3, 3, v3\n"
    "v_lshl_add_u32 v252, v2, 7, v3\n"
    "ds_read_b128 v[164:167], v248 offset:14336\n"
    "s_waitcnt lgkmcnt(3)\n"
    "v_mfma_f32_32x32x16_bf16 v[204:219], v[52:55], v[168:171], v[204:219]\n"
    "v_and_b32 v245, 0x3ff, v252\n"
    "v_add_u32 v252, 0x18000, v252\n"
    "s_lshr_b32 s64, s40, 1\n"
    "s_mul_i32 s64, s64, 12288\n"
    "ds_read_b128 v[168:171], v248 offset:15360\n"
    "s_waitcnt lgkmcnt(3)\n"
    "v_mfma_f32_32x32x16_bf16 v[204:219], v[56:59], v[156:159], v[204:219]\n"
    "s_mul_i32 s57, s67, 14\n"
    "s_add_u32 m0, s75, 0x1800\n"
    "s_add_u32 s57, s57, s76\n"
    "s_add_u32 s57, s57, 0x3800\n"
    "buffer_load_dwordx4 v244, s[24:27], s57 offen lds\n"
    "s_addk_i32 s64, 0x1000\n"
    "v_add_u32 v245, s64, v245\n"
    "v_mul_u32_u24 v2, 0x380, v2\n"
    "v_lshl_add_u32 v246, v197, 3, v2\n"
    "s_cmp_gt_u32 s40, 3\n"
    "s_cbranch_scc1 .Lfb_gr25\n"
    "s_waitcnt vmcnt(8)\n"
    "s_barrier\n"
    ".Lfb_gr25:\n"
    "ds_read_b128 v[156:159], v248 offset:16384\n"
    "s_waitcnt lgkmcnt(3)\n"
    "v_mfma_f32_32x32x16_bf16 v[204:219], v[60:63], v[160:163], v[204:219]\n"
    "s_mul_i32 s64, s43, 57344\n"
    "s_lshl_b32 s58, s44, 7\n"
    "s_add_u32 s77, s64, s58\n"
    "s_waitcnt vmcnt(7)\n"
    "v_xor_b32 v2, 0x80000000, v193\n"
    "ds_read_b128 v[160:163], v248 offset:17408\n"
    "s_waitcnt lgkmcnt(3)\n"
    "v_mfma_f32_32x32x16_bf16 v[204:219], v[64:67], v[164:167], v[204:219]\n"
    "s_waitcnt vmcnt(3)\n"
    "ds_read_b64_tr_b16 v[120:121], v203\n"
    "ds_read_b64_tr_b16 v[122:123], v203 offset:1024\n"
    "s_mul_i32 s57, s67, 14\n"
    "s_add_u32 m0, s75, 0x1c00\n"
    "s_add_u32 s57, s57, s76\n"
    "s_add_u32 s57, s57, 0x5400\n"
    "buffer_load_dwordx4 v244, s[24:27], s57 offen lds\n"
    "v_cvt_pk_bf16_f32 v152, v2, v2\n"
    "v_lshlrev_b32 v3, 16, v152\n"
    "v_sub_f32 v2, v2, v3\n"
    "v_cvt_pk_bf16_f32 v2, v2, 0\n"
    "ds_read_b128 v[164:167], v248 offset:18432\n"
    "s_waitcnt lgkmcnt(5)\n"
    "v_mfma_f32_32x32x16_bf16 v[204:219], v[68:71], v[168:171], v[204:219]\n"
    "ds_read_b64_tr_b16 v[124:125], v249\n"
    "ds_read_b64_tr_b16 v[126:127], v249 offset:1024\n"
    "v_lshl_or_b32 v153, v195, 16, v2\n"
    "v_cndmask_b32 v152, 0, v152, s[50:51]\n"
    "v_cndmask_b32 v153, 0, v153, s[50:51]\n"
    "v_cndmask_b32 v154, 0, v195, s[50:51]\n"
    "ds_read_b128 v[168:171], v248 offset:19456\n"
    "s_waitcnt lgkmcnt(7)\n"
    "v_mfma_f32_32x32x16_bf16 v[204:219], v[72:75], v[156:159], v[204:219]\n"
    "ds_read_b64_tr_b16 v[128:129], v203 offset:2048\n"
    "ds_read_b64_tr_b16 v[130:131], v203 offset:3072\n"
    "s_mul_i32 s57, s68, 14\n"
    "s_add_u32 m0, s75, 0x2000\n"
    "s_add_u32 s57, s57, s76\n"
    "buffer_load_dwordx4 v244, s[24:27], s57 offen lds\n"
    "v_mov_b32 v155, 0\n"
    "ds_read_b128 v[156:159], v248 offset:20480\n"
    "s_waitcnt lgkmcnt(9)\n"
    "v_mfma_f32_32x32x16_bf16 v[204:219], v[76:79], v[160:163], v[204:219]\n"
    "ds_read_b64_tr_b16 v[132:133], v249 offset:2048\n"
    "ds_read_b64_tr_b16 v[134:135], v249 offset:3072\n"
    "ds_read_b128 v[160:163], v248 offset:21504\n"
    "s_waitcnt lgkmcnt(9)\n"
    "v_mfma_f32_32x32x16_bf16 v[204:219], v[80:83], v[164:167], v[204:219]\n"
    "s_mul_i32 s57, s68, 14\n"
    "s_add_u32 m0, s75, 0x2400\n"
    "s_add_u32 s57, s57, s76\n"
    "s_add_u32 s57, s57, 0x1c00\n"
    "buffer_load_dwordx4 v244, s[24:27], s57 offen lds\n"
    "ds_read_b128 v[164:167], v248 offset:22528\n"
    "s_waitcnt lgkmcnt(7)\n"
    "v_mfma_f32_32x32x16_bf16 v[204:219], v[84:87], v[168:171], v[204:219]\n"
    "ds_read_b128 v[168:171], v248 offset:23552\n"
    "s_waitcnt lgkmcnt(5)\n"
    "v_mfma_f32_32x32x16_bf16 v[204:219], v[88:91], v[156:159], v[204:219]\n"
    "s_mul_i32 s57, s68, 14\n"
    "s_add_u32 m0, s75, 0x2800\n"
    "s_add_u32 s57, s57, s76\n"
    "s_add_u32 s57, s57, 0x3800\n"
    "buffer_load_dwordx4 v244, s[24:27], s57 offen lds\n"
    "ds_read_b128 v[156:159], v248 offset:24576\n"
    "s_waitcnt lgkmcnt(3)\n"
    "v_mfma_f32_32x32x16_bf16 v[204:219], v[92:95], v[160:163], v[204:219]\n"
    "ds_read_b128 v[160:163], v248 offset:25600\n"
    "s_waitcnt lgkmcnt(3)\n"
    "v_mfma_f32_32x32x16_bf16 v[204:219], v[96:99], v[164:167], v[204:219]\n"
    "s_waitcnt vmcnt(3)\n"
    "ds_read_b64_tr_b16 v[136:137], v203 offset:4096\n"
    "ds_read_b64_tr_b16 v[138:139], v203 offset:5120\n"
    "s_mul_i32 s57, s68, 14\n"
    "s_add_u32 m0, s75, 0x2c00\n"
    "s_add_u32 s57, s57, s76\n"
    "s_add_u32 s57, s57, 0x5400\n"
    "buffer_load_dwordx4 v244, s[24:27], s57 offen lds\n"
    "ds_read_b128 v[164:167], v248 offset:26624\n"
    "s_waitcnt lgkmcnt(5)\n"
    "v_mfma_f32_32x32x16_bf16 v[204:219], v[100:103], v[168:171], v[204:219]\n"
    "ds_read_b64_tr_b16 v[140:141], v249 offset:4096\n"
    "ds_read_b64_tr_b16 v[142:143], v249 offset:5120\n"
    "ds_read_b128 v[168:171], v248 offset:27648\n"
    "s_waitcnt lgkmcnt(7)\n"
    "v_mfma_f32_32x32x16_bf16 v[204:219], v[104:107], v[156:159], v[204:219]\n"
    "ds_read_b64_tr_b16 v[144:145], v203 offset:6144\n"
    "ds_read_b64_tr_b16 v[146:147], v203 offset:7168\n"
    "ds_read_b128 v[156:159], v248 offset:28672\n"
    "s_waitcnt lgkmcnt(9)\n"
    "v_mfma_f32_32x32x16_bf16 v[204:219], v[108:111], v[160:163], v[204:219]\n"
    "ds_read_b64_tr_b16 v[148:149], v249 offset:6144\n"
    "ds_read_b64_tr_b16 v[150:151], v249 offset:7168\n"
    "v_mov_b32 v242, v175\n"
    "v_cndmask_b32 v172, 0, v172, s[50:51]\n"
    "v_cndmask_b32 v173, 0, v173, s[50:51]\n"
    "v_cndmask_b32 v174, 0, v174, s[50:51]\n"
    "v_mov_b32 v175, 0\n"
    "ds_read_b128 v[160:163], v248 offset:29696\n"
    "s_waitcnt lgkmcnt(9)\n"
    "v_mfma_f32_32x32x16_bf16 v[204:219], v[112:115], v[164:167], v[204:219]\n"
    "ds_read_b128 v[164:167], v248 offset:30720\n"
    "s_waitcnt lgkmcnt(7)\n"
    "v_mfma_f32_32x32x16_bf16 v[204:219], v[116:119], v[168:171], v[204:219]\n"
    "v_mfma_f32_32x32x16_bf16 v[204:219], v[152:155], v[172:175], v[204:219]\n"
    "s_setprio 1\n"
    "s_cmp_gt_u32 s40, 3\n"
    "s_cbranch_scc1 .Lfb_pr26\n"
    "s_setprio 0\n"
    ".Lfb_pr26:\n"
    "s_nop 8\n"
    "v_mul_f32 v220, v242, v204\n"
    "v_mul_f32 v226, v242, v206\n"
    "v_mul_f32 v221, v242, v205\n"
    "v_mul_f32 v227, v242, v207\n"
    "v_exp_f32 v222, v220\n"
    "v_exp_f32 v228, v226\n"
    "v_exp_f32 v223, v221\n"
    "v_exp_f32 v229, v227\n"
    "v_add_f32 v224, 2.0, v222\n"
    "v_add_f32 v230, 2.0, v228\n"
    "v_add_f32 v225, 2.0, v223\n"
    "v_add_f32 v231, 2.0, v229\n"
    "v_fma_f32 v224, v222, v224, 2.0\n"
    "v_fma_f32 v230, v228, v230, 2.0\n"
    "v_fma_f32 v225, v223, v225, 2.0\n"
    "v_fma_f32 v231, v229, v231, 2.0\n"
    "v_rcp_f32 v224, v224\n"
    "v_rcp_f32 v230, v230\n"
    "v_rcp_f32 v225, v225\n"
    "v_rcp_f32 v231, v231\n"
    "v_fma_f32 v224, v224, v240, v241\n"
    "v_fma_f32 v230, v230, v240, v241\n"
    "v_fma_f32 v225, v225, v240, v241\n"
    "v_fma_f32 v231, v231, v240, v241\n"
    "v_mul_f32 v220, v220, v224\n"
    "v_mul_f32 v226, v226, v230\n"
    "v_mul_f32 v221, v221, v225\n"
    "v_mul_f32 v227, v227, v231\n"
    "v_cvt_pk_bf16_f32 v180, v220, v221\n"
    "v_cvt_pk_bf16_f32 v181, v226, v227\n"
    "v_mul_f32 v220, v242, v208\n"
    "v_mul_f32 v226, v242, v210\n"
    "v_mul_f32 v221, v242, v209\n"
    "v_mul_f32 v227, v242, v211\n"
    "v_exp_f32 v222, v220\n"
    "v_exp_f32 v228, v226\n"
    "v_exp_f32 v223, v221\n"
    "v_exp_f32 v229, v227\n"
    "v_add_f32 v224, 2.0, v222\n"
    "v_add_f32 v230, 2.0, v228\n"
    "v_add_f32 v225, 2.0, v223\n"
    "v_add_f32 v231, 2.0, v229\n"
    "v_fma_f32 v224, v222, v224, 2.0\n"
    "v_fma_f32 v230, v228, v230, 2.0\n"
    "v_fma_f32 v225, v223, v225, 2.0\n"
    "v_fma_f32 v231, v229, v231, 2.0\n"
    "v_rcp_f32 v224, v224\n"
    "v_rcp_f32 v230, v230\n"
    "v_rcp_f32 v225, v225\n"
    "v_rcp_f32 v231, v231\n"
    "v_fma_f32 v224, v224, v240, v241\n"
    "v_fma_f32 v230, v230, v240, v241\n"
    "v_fma_f32 v225, v225, v240, v241\n"
    "v_fma_f32 v231, v231, v240, v241\n"
    "v_mul_f32 v220, v220, v224\n"
    "v_mul_f32 v226, v226, v230\n"
    "v_mul_f32 v221, v221, v225\n"
    "v_mul_f32 v227, v227, v231\n"
    "v_cvt_pk_bf16_f32 v182, v220, v221\n"
    "v_cvt_pk_bf16_f32 v183, v226, v227\n"
    "buffer_store_dwordx4 v[180:183], v1, s[12:15], s59 offen sc1\n"
    "v_mul_f32 v220, v242, v212\n"
    "v_mul_f32 v226, v242, v214\n"
    "v_mul_f32 v221, v242, v213\n"
    "v_mul_f32 v227, v242, v215\n"
    "v_exp_f32 v222, v220\n"
    "v_exp_f32 v228, v226\n"
    "v_exp_f32 v223, v221\n"
    "v_exp_f32 v229, v227\n"
    "v_add_f32 v224, 2.0, v222\n"
    "v_add_f32 v230, 2.0, v228\n"
    "v_add_f32 v225, 2.0, v223\n"
    "v_add_f32 v231, 2.0, v229\n"
    "v_fma_f32 v224, v222, v224, 2.0\n"
    "v_fma_f32 v230, v228, v230, 2.0\n"
    "v_fma_f32 v225, v223, v225, 2.0\n"
    "v_fma_f32 v231, v229, v231, 2.0\n"
    "v_rcp_f32 v224, v224\n"
    "v_rcp_f32 v230, v230\n"
    "v_rcp_f32 v225, v225\n"
    "v_rcp_f32 v231, v231\n"
    "v_fma_f32 v224, v224, v240, v241\n"
    "v_fma_f32 v230, v230, v240, v241\n"
    "v_fma_f32 v225, v225, v240, v241\n"
    "v_fma_f32 v231, v231, v240, v241\n"
    "v_mul_f32 v220, v220, v224\n"
    "v_mul_f32 v226, v226, v230\n"
    "v_mul_f32 v221, v221, v225\n"
    "v_mul_f32 v227, v227, v231\n"
    "v_cvt_pk_bf16_f32 v184, v220, v221\n"
    "v_cvt_pk_bf16_f32 v185, v226, v227\n"
    "v_mul_f32 v220, v242, v216\n"
    "v_mul_f32 v226, v242, v218\n"
    "v_mul_f32 v221, v242, v217\n"
    "v_mul_f32 v227, v242, v219\n"
    "v_exp_f32 v222, v220\n"
    "v_exp_f32 v228, v226\n"
    "v_exp_f32 v223, v221\n"
    "v_exp_f32 v229, v227\n"
    "v_add_f32 v224, 2.0, v222\n"
    "v_add_f32 v230, 2.0, v228\n"
    "v_add_f32 v225, 2.0, v223\n"
    "v_add_f32 v231, 2.0, v229\n"
    "v_fma_f32 v224, v222, v224, 2.0\n"
    "v_fma_f32 v230, v228, v230, 2.0\n"
    "v_fma_f32 v225, v223, v225, 2.0\n"
    "v_fma_f32 v231, v229, v231, 2.0\n"
    "v_rcp_f32 v224, v224\n"
    "v_rcp_f32 v230, v230\n"
    "v_rcp_f32 v225, v225\n"
    "v_rcp_f32 v231, v231\n"
    "v_fma_f32 v224, v224, v240, v241\n"
    "v_fma_f32 v230, v230, v240, v241\n"
    "v_fma_f32 v225, v225, v240, v241\n"
    "v_fma_f32 v231, v231, v240, v241\n"
    "v_mul_f32 v220, v220, v224\n"
    "v_mul_f32 v226, v226, v230\n"
    "v_mul_f32 v221, v221, v225\n"
    "v_mul_f32 v227, v227, v231\n"
    "v_cvt_pk_bf16_f32 v186, v220, v221\n"
    "v_cvt_pk_bf16_f32 v187, v226, v227\n"
    "buffer_store_dwordx4 v[184:187], v1, s[12:15], s59 offen offset:1024 sc1\n"
    "s_add_u32 s59, s59, 0x1c000\n"
    "ds_read_b128 v[168:171], v248 offset:31744\n"
    "s_waitcnt lgkmcnt(5)\n"
    "v_mfma_f32_32x32x16_bf16 v[204:219], v[8:11], v[156:159], 0\n"
    "ds_read_b128 v[156:159], v248 offset:32768\n"
    "s_waitcnt lgkmcnt(3)\n"
    "v_mfma_f32_32x32x16_bf16 v[204:219], v[12:15], v[160:163], v[204:219]\n"
    "ds_read_b128 v[160:163], v248 offset:33792\n"
    "s_waitcnt lgkmcnt(3)\n"
    "v_mfma_f32_32x32x16_bf16 v[204:219], v[16:19], v[164:167], v[204:219]\n"
    "s_mul_i32 s57, s69, 14\n"
    "s_add_u32 m0, s75, 0x0\n"
    "s_add_u32 s57, s57, s76\n"
    "buffer_load_dwordx4 v244, s[24:27], s57 offen lds\n"
    "ds_read_b128 v[164:167], v248 offset:34816\n"
    "s_waitcnt lgkmcnt(3)\n"
    "v_mfma_f32_32x32x16_bf16 v[204:219], v[20:23], v[168:171], v[204:219]\n"
    "ds_read_b128 v[168:171], v248 offset:35840\n"
    "s_waitcnt lgkmcnt(3)\n"
    "v_mfma_f32_32x32x16_bf16 v[204:219], v[24:27], v[156:159], v[204:219]\n"
    "s_waitcnt vmcnt(3)\n"
    "ds_read_b64_tr_b16 v[8:9], v203 offset:8192\n"
    "ds_read_b64_tr_b16 v[10:11], v203 offset:9216\n"
    "s_mul_i32 s57, s69, 14\n"
    "s_add_u32 m0, s75, 0x400\n"
    "s_add_u32 s57, s57, s76\n"
    "s_add_u32 s57, s57, 0x1c00\n"
    "buffer_load_dwordx4 v244, s[24:27], s57 offen lds\n"
    "ds_read_b128 v[156:159], v248 offset:36864\n"
    "s_waitcnt lgkmcnt(5)\n"
    "v_mfma_f32_32x32x16_bf16 v[204:219], v[28:31], v[160:163], v[204:219]\n"
    "ds_read_b64_tr_b16 v[12:13], v249 offset:8192\n"
    "ds_read_b64_tr_b16 v[14:15], v249 offset:9216\n"
    "ds_read_b128 v[160:163], v248 offset:37888\n"
    "s_waitcnt lgkmcnt(7)\n"
    "v_mfma_f32_32x32x16_bf16 v[204:219], v[32:35], v[164:167], v[204:219]\n"
    "ds_read_b64_tr_b16 v[16:17], v203 offset:10240\n"
    "ds_read_b64_tr_b16 v[18:19], v203 offset:11264\n"
    "s_mul_i32 s57, s69, 14\n"
    "s_add_u32 m0, s75, 0x800\n"
    "s_add_u32 s57, s57, s76\n"
    "s_add_u32 s57, s57, 0x3800\n"
    "buffer_load_dwordx4 v244, s[24:27], s57 offen lds\n"
    "ds_read_b128 v[164:167], v248 offset:38912\n"
    "s_waitcnt lgkmcnt(9)\n"
    "v_mfma_f32_32x32x16_bf16 v[204:219], v[36:39], v[168:171], v[204:219]\n"
    "ds_read_b64_tr_b16 v[20:21], v249 offset:10240\n"
    "ds_read_b64_tr_b16 v[22:23], v249 offset:11264\n"
    "ds_read_b128 v[168:171], v248 offset:39936\n"
    "s_waitcnt lgkmcnt(9)\n"
    "v_mfma_f32_32x32x16_bf16 v[204:219], v[40:43], v[156:159], v[204:219]\n"
    "s_mul_i32 s57, s69, 14\n"
    "s_add_u32 m0, s75, 0xc00\n"
    "s_add_u32 s57, s57, s76\n"
    "s_add_u32 s57, s57, 0x5400\n"
    "buffer_load_dwordx4 v244, s[24:27], s57 offen lds\n"
    "ds_read_b128 v[156:159], v248 offset:40960\n"
    "s_waitcnt lgkmcnt(7)\n"
    "v_mfma_f32_32x32x16_bf16 v[204:219], v[44:47], v[160:163], v[204:219]\n"
    "ds_read_b128 v[160:163], v248 offset:41984\n"
    "s_waitcnt lgkmcnt(5)\n"
    "v_mfma_f32_32x32x16_bf16 v[204:219], v[48:51], v[164:167], v[204:219]\n"
    "s_mul_i32 s57, s70, 14\n"
    "s_add_u32 m0, s75, 0x1000\n"
    "s_add_u32 s57, s57, s76\n"
    "buffer_load_dwordx4 v244, s[24:27], s57 offen lds\n"
    "ds_read_b128 v[164:167], v248 offset:43008\n"
    "s_waitcnt lgkmcnt(3)\n"
    "v_mfma_f32_32x32x16_bf16 v[204:219], v[52:55], v[168:171], v[204:219]\n"
    "ds_read_b128 v[168:171], v248 offset:44032\n"
    "s_waitcnt lgkmcnt(3)\n"
    "v_mfma_f32_32x32x16_bf16 v[204:219], v[56:59], v[156:159], v[204:219]\n"
    "s_mul_i32 s57, s70, 14\n"
    "s_add_u32 m0, s75, 0x1400\n"
    "s_add_u32 s57, s57, s76\n"
    "s_add_u32 s57, s57, 0x1c00\n"
    "buffer_load_dwordx4 v244, s[24:27], s57 offen lds\n"
    "ds_read_b128 v[156:159], v248 offset:45056\n"
    "s_waitcnt lgkmcnt(3)\n"
    "v_mfma_f32_32x32x16_bf16 v[204:219], v[60:63], v[160:163], v[204:219]\n"
    "ds_read_b128 v[160:163], v248 offset:46080\n"
    "s_waitcnt lgkmcnt(3)\n"
    "v_mfma_f32_32x32x16_bf16 v[204:219], v[64:67], v[164:167], v[204:219]\n"
    "s_mul_i32 s57, s70, 14\n"
    "s_add_u32 m0, s75, 0x1800\n"
    "s_add_u32 s57, s57, s76\n"
    "s_add_u32 s57, s57, 0x3800\n"
    "buffer_load_dwordx4 v244, s[24:27], s57 offen lds\n"
    "ds_read_b128 v[164:167], v248 offset:47104\n"
    "s_waitcnt lgkmcnt(3)\n"
    "v_mfma_f32_32x32x16_bf16 v[204:219], v[68:71], v[168:171], v[204:219]\n"
    "ds_read_b128 v[168:171], v248 offset:48128\n"
    "s_waitcnt lgkmcnt(3)\n"
    "v_mfma_f32_32x32x16_bf16 v[204:219], v[72:75], v[156:159], v[204:219]\n"
    "s_waitcnt vmcnt(3)\n"
    "ds_read_b64_tr_b16 v[24:25], v203\n"
    "ds_read_b64_tr_b16 v[26:27], v203 offset:1024\n"
    "s_mul_i32 s57, s70, 14\n"
    "s_add_u32 m0, s75, 0x1c00\n"
    "s_add_u32 s57, s57, s76\n"
    "s_add_u32 s57, s57, 0x5400\n"
    "buffer_load_dwordx4 v244, s[24:27], s57 offen lds\n"
    "v_mov_b32 v2, 1\n"
    "s_mov_b64 exec, 1\n"
    "ds_add_rtn_u32 v3, v255, v2\n"
    "s_waitcnt lgkmcnt(0)\n"
    "v_readfirstlane_b32 s56, v3\n"
    "s_cmp_lg_u32 s56, 7\n"
    "s_cbranch_scc1 .Lfb_sg27\n"
    "s_add_u32 s56, s55, 0x5000\n"
    "buffer_store_dwordx2 v[6:7], off, s[12:15], s56 sc1\n"
    ".Lfb_sg27:\n"
    "s_mov_b64 exec, -1\n"
    "ds_read_b128 v[156:159], v248 offset:49152\n"
    "v_mfma_f32_32x32x16_bf16 v[204:219], v[76:79], v[160:163], v[204:219]\n"
    "ds_read_b64_tr_b16 v[28:29], v249\n"
    "ds_read_b64_tr_b16 v[30:31], v249 offset:1024\n"
    "ds_read_b128 v[160:163], v248 offset:50176\n"
    "v_mfma_f32_32x32x16_bf16 v[204:219], v[80:83], v[164:167], v[204:219]\n"
    "ds_read_b64_tr_b16 v[32:33], v203 offset:2048\n"
    "ds_read_b64_tr_b16 v[34:35], v203 offset:3072\n"
    "s_mul_i32 s57, s71, 14\n"
    "s_add_u32 m0, s75, 0x2000\n"
    "s_add_u32 s57, s57, s76\n"
    "buffer_load_dwordx4 v244, s[24:27], s57 offen lds\n"
    "ds_read_b128 v[164:167], v248 offset:51200\n"
    "v_mfma_f32_32x32x16_bf16 v[204:219], v[84:87], v[168:171], v[204:219]\n"
    "ds_read_b64_tr_b16 v[36:37], v249 offset:2048\n"
    "ds_read_b64_tr_b16 v[38:39], v249 offset:3072\n"
    "ds_read_b128 v[168:171], v248 offset:52224\n"
    "s_waitcnt lgkmcnt(9)\n"
    "v_mfma_f32_32x32x16_bf16 v[204:219], v[88:91], v[156:159], v[204:219]\n"
    "s_mul_i32 s57, s71, 14\n"
    "s_add_u32 m0, s75, 0x2400\n"
    "s_add_u32 s57, s57, s76\n"
    "s_add_u32 s57, s57, 0x1c00\n"
    "buffer_load_dwordx4 v244, s[24:27], s57 offen lds\n"
    "ds_read_b128 v[156:159], v248 offset:53248\n"
    "s_waitcnt lgkmcnt(7)\n"
    "v_mfma_f32_32x32x16_bf16 v[204:219], v[92:95], v[160:163], v[204:219]\n"
    "ds_read_b128 v[160:163], v248 offset:54272\n"
    "s_waitcnt lgkmcnt(5)\n"
    "v_mfma_f32_32x32x16_bf16 v[204:219], v[96:99], v[164:167], v[204:219]\n"
    "s_mul_i32 s57, s71, 14\n"
    "s_add_u32 m0, s75, 0x2800\n"
    "s_add_u32 s57, s57, s76\n"
    "s_add_u32 s57, s57, 0x3800\n"
    "buffer_load_dwordx4 v244, s[24:27], s57 offen lds\n"
    "ds_read_b128 v[164:167], v248 offset:55296\n"
    "s_waitcnt lgkmcnt(3)\n"
    "v_mfma_f32_32x32x16_bf16 v[204:219], v[100:103], v[168:171], v[204:219]\n"
    "ds_read_b128 v[168:171], v248 offset:56320\n"
    "s_waitcnt lgkmcnt(3)\n"
    "v_mfma_f32_32x32x16_bf16 v[204:219], v[104:107], v[156:159], v[204:219]\n"
    "s_waitcnt vmcnt(3)\n"
    "ds_read_b64_tr_b16 v[40:41], v203 offset:4096\n"
    "ds_read_b64_tr_b16 v[42:43], v203 offset:5120\n"
    "s_mul_i32 s57, s71, 14\n"
    "s_add_u32 m0, s75, 0x2c00\n"
    "s_add_u32 s57, s57, s76\n"
    "s_add_u32 s57, s57, 0x5400\n"
    "buffer_load_dwordx4 v244, s[24:27], s57 offen lds\n"
    "s_waitcnt lgkmcnt(4)\n"
    "v_mfma_f32_32x32x16_bf16 v[204:219], v[108:111], v[160:163], v[204:219]\n"
    "ds_read_b64_tr_b16 v[44:45], v249 offset:4096\n"
    "ds_read_b64_tr_b16 v[46:47], v249 offset:5120\n"
    "v_mov_b32 v242, v179\n"
    "v_cndmask_b32 v176, 0, v176, s[50:51]\n"
    "v_cndmask_b32 v177, 0, v177, s[50:51]\n"
    "v_cndmask_b32 v178, 0, v178, s[50:51]\n"
    "v_mov_b32 v179, 0\n"
    "s_waitcnt lgkmcnt(5)\n"
    "v_mfma_f32_32x32x16_bf16 v[204:219], v[112:115], v[164:167], v[204:219]\n"
    "ds_read_b64_tr_b16 v[48:49], v203 offset:6144\n"
    "ds_read_b64_tr_b16 v[50:51], v203 offset:7168\n"
    "s_waitcnt lgkmcnt(6)\n"
    "v_mfma_f32_32x32x16_bf16 v[204:219], v[116:119], v[168:171], v[204:219]\n"
    "ds_read_b64_tr_b16 v[52:53], v249 offset:6144\n"
    "ds_read_b64_tr_b16 v[54:55], v249 offset:7168\n"
    "v_mfma_f32_32x32x16_bf16 v[204:219], v[152:155], v[176:179], v[204:219]\n"
    "s_nop 11\n"
    "v_mul_f32 v220, v242, v204\n"
    "v_mul_f32 v226, v242, v206\n"
    "v_mul_f32 v221, v242, v205\n"
    "v_mul_f32 v227, v242, v207\n"
    "v_exp_f32 v222, v220\n"
    "v_exp_f32 v228, v226\n"
    "v_exp_f32 v223, v221\n"
    "v_exp_f32 v229, v227\n"
    "v_add_f32 v224, 2.0, v222\n"
    "v_add_f32 v230, 2.0, v228\n"
    "v_add_f32 v225, 2.0, v223\n"
    "v_add_f32 v231, 2.0, v229\n"
    "v_fma_f32 v224, v222, v224, 2.0\n"
    "v_fma_f32 v230, v228, v230, 2.0\n"
    "v_fma_f32 v225, v223, v225, 2.0\n"
    "v_fma_f32 v231, v229, v231, 2.0\n"
    "v_rcp_f32 v224, v224\n"
    "v_rcp_f32 v230, v230\n"
    "v_rcp_f32 v225, v225\n"
    "v_rcp_f32 v231, v231\n"
    "v_fma_f32 v224, v224, v240, v241\n"
    "v_fma_f32 v230, v230, v240, v241\n"
    "v_fma_f32 v225, v225, v240, v241\n"
    "v_fma_f32 v231, v231, v240, v241\n"
    "v_mul_f32 v220, v220, v224\n"
    "v_mul_f32 v226, v226, v230\n"
    "v_mul_f32 v221, v221, v225\n"
    "v_mul_f32 v227, v227, v231\n"
    "v_cvt_pk_bf16_f32 v188, v220, v221\n"
    "v_cvt_pk_bf16_f32 v189, v226, v227\n"
    "v_mul_f32 v220, v242, v208\n"
    "v_mul_f32 v226, v242, v210\n"
    "v_mul_f32 v221, v242, v209\n"
    "v_mul_f32 v227, v242, v211\n"
    "v_exp_f32 v222, v220\n"
    "v_exp_f32 v228, v226\n"
    "v_exp_f32 v223, v221\n"
    "v_exp_f32 v229, v227\n"
    "v_add_f32 v224, 2.0, v222\n"
    "v_add_f32 v230, 2.0, v228\n"
    "v_add_f32 v225, 2.0, v223\n"
    "v_add_f32 v231, 2.0, v229\n"
    "v_fma_f32 v224, v222, v224, 2.0\n"
    "v_fma_f32 v230, v228, v230, 2.0\n"
    "v_fma_f32 v225, v223, v225, 2.0\n"
    "v_fma_f32 v231, v229, v231, 2.0\n"
    "v_rcp_f32 v224, v224\n"
    "v_rcp_f32 v230, v230\n"
    "v_rcp_f32 v225, v225\n"
    "v_rcp_f32 v231, v231\n"
    "v_fma_f32 v224, v224, v240, v241\n"
    "v_fma_f32 v230, v230, v240, v241\n"
    "v_fma_f32 v225, v225, v240, v241\n"
    "v_fma_f32 v231, v231, v240, v241\n"
    "v_mul_f32 v220, v220, v224\n"
    "v_mul_f32 v226, v226, v230\n"
    "v_mul_f32 v221, v221, v225\n"
    "v_mul_f32 v227, v227, v231\n"
    "v_cvt_pk_bf16_f32 v190, v220, v221\n"
    "v_cvt_pk_bf16_f32 v191, v226, v227\n"
    "buffer_store_dwordx4 v[188:191], v1, s[12:15], s59 offen sc1\n"
    "v_mul_f32 v220, v242, v212\n"
    "v_mul_f32 v226, v242, v214\n"
    "v_mul_f32 v221, v242, v213\n"
    "v_mul_f32 v227, v242, v215\n"
    "v_exp_f32 v222, v220\n"
    "v_exp_f32 v228, v226\n"
    "v_exp_f32 v223, v221\n"
    "v_exp_f32 v229, v227\n"
    "v_add_f32 v224, 2.0, v222\n"
    "v_add_f32 v230, 2.0, v228\n"
    "v_add_f32 v225, 2.0, v223\n"
    "v_add_f32 v231, 2.0, v229\n"
    "v_fma_f32 v224, v222, v224, 2.0\n"
    "v_fma_f32 v230, v228, v230, 2.0\n"
    "v_fma_f32 v225, v223, v225, 2.0\n"
    "v_fma_f32 v231, v229, v231, 2.0\n"
    "v_rcp_f32 v224, v224\n"
    "v_rcp_f32 v230, v230\n"
    "v_rcp_f32 v225, v225\n"
    "v_rcp_f32 v231, v231\n"
    "v_fma_f32 v224, v224, v240, v241\n"
    "v_fma_f32 v230, v230, v240, v241\n"
    "v_fma_f32 v225, v225, v240, v241\n"
    "v_fma_f32 v231, v231, v240, v241\n"
    "v_mul_f32 v220, v220, v224\n"
    "v_mul_f32 v226, v226, v230\n"
    "v_mul_f32 v221, v221, v225\n"
    "v_mul_f32 v227, v227, v231\n"
    "v_cvt_pk_bf16_f32 v192, v220, v221\n"
    "v_cvt_pk_bf16_f32 v193, v226, v227\n"
    "v_mul_f32 v220, v242, v216\n"
    "v_mul_f32 v226, v242, v218\n"
    "v_mul_f32 v221, v242, v217\n"
    "v_mul_f32 v227, v242, v219\n"
    "v_exp_f32 v222, v220\n"
    "v_exp_f32 v228, v226\n"
    "v_exp_f32 v223, v221\n"
    "v_exp_f32 v229, v227\n"
    "v_add_f32 v224, 2.0, v222\n"
    "v_add_f32 v230, 2.0, v228\n"
    "v_add_f32 v225, 2.0, v223\n"
    "v_add_f32 v231, 2.0, v229\n"
    "v_fma_f32 v224, v222, v224, 2.0\n"
    "v_fma_f32 v230, v228, v230, 2.0\n"
    "v_fma_f32 v225, v223, v225, 2.0\n"
    "v_fma_f32 v231, v229, v231, 2.0\n"
    "v_rcp_f32 v224, v224\n"
    "v_rcp_f32 v230, v230\n"
    "v_rcp_f32 v225, v225\n"
    "v_rcp_f32 v231, v231\n"
    "v_fma_f32 v224, v224, v240, v241\n"
    "v_fma_f32 v230, v230, v240, v241\n"
    "v_fma_f32 v225, v225, v240, v241\n"
    "v_fma_f32 v231, v231, v240, v241\n"
    "v_mul_f32 v220, v220, v224\n"
    "v_mul_f32 v226, v226, v230\n"
    "v_mul_f32 v221, v221, v225\n"
    "v_mul_f32 v227, v227, v231\n"
    "v_cvt_pk_bf16_f32 v194, v220, v221\n"
    "v_cvt_pk_bf16_f32 v195, v226, v227\n"
    "buffer_store_dwordx4 v[192:195], v1, s[12:15], s59 offen offset:1024 sc1\n"
    "s_cmp_lg_u32 s40, 4\n"
    "s_cbranch_scc1 .Lfb_pa28\n"
    "s_cmp_lg_u32 s40, 4\n"
    "s_cbranch_scc1 .Lfb_np30\n"
    "s_add_u32 s56, s54, 0x5000\n"
    "s_mov_b32 s64, 0x30d40\n"
    ".Lfb_pl31:\n"
    "buffer_load_dwordx2 v[2:3], v250, s[12:15], s56 offen sc1\n"
    "s_waitcnt vmcnt(0)\n"
    "v_cmp_ne_u64 vcc, v[2:3], s[46:47]\n"
    "s_cbranch_vccz .Lfb_np30\n"
    "s_sleep 1\n"
    "s_sub_u32 s64, s64, 1\n"
    "s_cbranch_scc0 .Lfb_pl31\n"
    ".Lfb_np30:\n"
    "s_barrier\n"
    "s_mul_i32 s57, s72, 14\n"
    "s_add_u32 m0, s75, 0x0\n"
    "s_add_u32 s57, s57, s76\n"
    "buffer_load_dwordx4 v244, s[24:27], s57 offen lds\n"
    "s_mul_i32 s57, s72, 14\n"
    "s_add_u32 m0, s75, 0x400\n"
    "s_add_u32 s57, s57, s76\n"
    "s_add_u32 s57, s57, 0x1c00\n"
    "buffer_load_dwordx4 v244, s[24:27], s57 offen lds\n"
    "s_mul_i32 s57, s72, 14\n"
    "s_add_u32 m0, s75, 0x800\n"
    "s_add_u32 s57, s57, s76\n"
    "s_add_u32 s57, s57, 0x3800\n"
    "buffer_load_dwordx4 v244, s[24:27], s57 offen lds\n"
    "s_mul_i32 s57, s72, 14\n"
    "s_add_u32 m0, s75, 0xc00\n"
    "s_add_u32 s57, s57, s76\n"
    "s_add_u32 s57, s57, 0x5400\n"
    "buffer_load_dwordx4 v244, s[24:27], s57 offen lds\n"
    "s_branch .Lfb_pb29\n"
    ".Lfb_pa28:\n"
    "s_mul_i32 s57, s72, 14\n"
    "s_add_u32 m0, s75, 0x0\n"
    "s_add_u32 s57, s57, s76\n"
    "buffer_load_dwordx4 v244, s[24:27], s57 offen lds\n"
    "s_mul_i32 s57, s72, 14\n"
    "s_add_u32 m0, s75, 0x400\n"
    "s_add_u32 s57, s57, s76\n"
    "s_add_u32 s57, s57, 0x1c00\n"
    "buffer_load_dwordx4 v244, s[24:27], s57 offen lds\n"
    "s_mul_i32 s57, s72, 14\n"
    "s_add_u32 m0, s75, 0x800\n"
    "s_add_u32 s57, s57, s76\n"
    "s_add_u32 s57, s57, 0x3800\n"
    "buffer_load_dwordx4 v244, s[24:27], s57 offen lds\n"
    "s_mul_i32 s57, s72, 14\n"
    "s_add_u32 m0, s75, 0xc00\n"
    "s_add_u32 s57, s57, s76\n"
    "s_add_u32 s57, s57, 0x5400\n"
    "buffer_load_dwordx4 v244, s[24:27], s57 offen lds\n"
    "s_waitcnt lgkmcnt(0)\n"
    "s_barrier\n"
    ".Lfb_pb29:\n"
    "s_waitcnt vmcnt(6)\n"
    "ds_read_b64_tr_b16 v[56:57], v203 offset:8192\n"
    "ds_read_b64_tr_b16 v[58:59], v203 offset:9216\n"
    "ds_read_b64_tr_b16 v[60:61], v249 offset:8192\n"
    "ds_read_b64_tr_b16 v[62:63], v249 offset:9216\n"
    "ds_read_b64_tr_b16 v[64:65], v203 offset:10240\n"
    "ds_read_b64_tr_b16 v[66:67], v203 offset:11264\n"
    "ds_read_b64_tr_b16 v[68:69], v249 offset:10240\n"
    "ds_read_b64_tr_b16 v[70:71], v249 offset:11264\n"
    "v_xor_b32 v2, 16, v253\n"
    "ds_write_b64 v253, v[236:237]\n"
    "ds_write_b64 v2, v[238:239]\n"
    "v_mfma_f32_32x32x16_bf16 v[204:219], v[120:123], v[180:183], 0\n"
    "v_mfma_f32_32x32x16_bf16 v[220:235], v[124:127], v[180:183], 0\n"
    "v_mfma_f32_32x32x16_bf16 v[204:219], v[128:131], v[184:187], v[204:219]\n"
    "v_mfma_f32_32x32x16_bf16 v[220:235], v[132:135], v[184:187], v[220:235]\n"
    "s_waitcnt vmcnt(4)\n"
    "v_mov_b32 v2, 1\n"
    "s_mov_b64 exec, 1\n"
    "ds_add_rtn_u32 v3, v255, v2 offset:4\n"
    "s_waitcnt lgkmcnt(0)\n"
    "v_readfirstlane_b32 s56, v3\n"
    "s_cmp_lg_u32 s56, 7\n"
    "s_cbranch_scc1 .Lfb_sg32\n"
    "s_add_u32 s56, s55, 0x5100\n"
    "buffer_store_dwordx2 v[6:7], off, s[12:15], s56 sc1\n"
    ".Lfb_sg32:\n"
    "s_mov_b64 exec, -1\n"
    "s_add_u32 s56, s73, s67\n"
    "buffer_load_dwordx4 v[88:91], v1, s[12:15], s56 offen sc1\n"
    "buffer_load_dwordx4 v[92:95], v1, s[12:15], s56 offen offset:1024 sc1\n"
    "s_add_u32 s56, s73, s68\n"
    "buffer_load_dwordx4 v[96:99], v1, s[12:15], s56 offen sc1\n"
    "buffer_load_dwordx4 v[100:103], v1, s[12:15], s56 offen offset:1024 sc1\n"
    "s_add_u32 s56, s73, s69\n"
    "buffer_load_dwordx4 v[104:107], v1, s[12:15], s56 offen sc1\n"
    "buffer_load_dwordx4 v[108:111], v1, s[12:15], s56 offen offset:1024 sc1\n"
    "s_add_u32 s56, s73, s70\n"
    "buffer_load_dwordx4 v[112:115], v1, s[12:15], s56 offen sc1\n"
    "buffer_load_dwordx4 v[116:119], v1, s[12:15], s56 offen offset:1024 sc1\n"
    "s_add_u32 s56, s73, s71\n"
    "buffer_load_dwordx4 v[156:159], v1, s[12:15], s56 offen sc1\n"
    "buffer_load_dwordx4 v[160:163], v1, s[12:15], s56 offen offset:1024 sc1\n"
    "s_add_u32 s56, s73, s72\n"
    "buffer_load_dwordx4 v[164:167], v1, s[12:15], s56 offen sc1\n"
    "buffer_load_dwordx4 v[168:171], v1, s[12:15], s56 offen offset:1024 sc1\n"
    "s_waitcnt vmcnt(11)\n"
    "v_mfma_f32_32x32x16_bf16 v[204:219], v[136:139], v[88:91], v[204:219]\n"
    "v_mfma_f32_32x32x16_bf16 v[220:235], v[140:143], v[88:91], v[220:235]\n"
    "s_waitcnt vmcnt(10)\n"
    "v_mfma_f32_32x32x16_bf16 v[204:219], v[144:147], v[92:95], v[204:219]\n"
    "v_mfma_f32_32x32x16_bf16 v[220:235], v[148:151], v[92:95], v[220:235]\n"
    "s_waitcnt vmcnt(9)\n"
    "v_mfma_f32_32x32x16_bf16 v[204:219], v[8:11], v[96:99], v[204:219]\n"
    "ds_read_b64_tr_b16 v[72:73], v203\n"
    "ds_read_b64_tr_b16 v[74:75], v203 offset:1024\n"
    "v_mfma_f32_32x32x16_bf16 v[220:235], v[12:15], v[96:99], v[220:235]\n"
    "ds_read_b64_tr_b16 v[76:77], v249\n"
    "ds_read_b64_tr_b16 v[78:79], v249 offset:1024\n"
    "s_waitcnt vmcnt(8)\n"
    "v_mfma_f32_32x32x16_bf16 v[204:219], v[16:19], v[100:103], v[204:219]\n"
    "ds_read_b64_tr_b16 v[80:81], v203 offset:2048\n"
    "ds_read_b64_tr_b16 v[82:83], v203 offset:3072\n"
    "v_mfma_f32_32x32x16_bf16 v[220:235], v[20:23], v[100:103], v[220:235]\n"
    "ds_read_b64_tr_b16 v[84:85], v249 offset:2048\n"
    "ds_read_b64_tr_b16 v[86:87], v249 offset:3072\n"
    "s_waitcnt vmcnt(7)\n"
    "v_mfma_f32_32x32x16_bf16 v[204:219], v[24:27], v[104:107], v[204:219]\n"
    "v_mfma_f32_32x32x16_bf16 v[220:235], v[28:31], v[104:107], v[220:235]\n"
    "s_waitcnt vmcnt(6)\n"
    "v_mfma_f32_32x32x16_bf16 v[204:219], v[32:35], v[108:111], v[204:219]\n"
    "v_mfma_f32_32x32x16_bf16 v[220:235], v[36:39], v[108:111], v[220:235]\n"
    "v_mov_b32 v203, 0x3f80\n"
    "v_mov_b32 v249, 0x3f800000\n"
    "s_waitcnt vmcnt(5)\n"
    "v_mfma_f32_32x32x16_bf16 v[204:219], v[40:43], v[112:115], v[204:219]\n"
    "v_mfma_f32_32x32x16_bf16 v[220:235], v[44:47], v[112:115], v[220:235]\n"
    "s_waitcnt vmcnt(4)\n"
    "v_mfma_f32_32x32x16_bf16 v[204:219], v[48:51], v[116:119], v[204:219]\n"
    "v_mfma_f32_32x32x16_bf16 v[220:235], v[52:55], v[116:119], v[220:235]\n"
    "s_waitcnt vmcnt(3)\n"
    "v_mfma_f32_32x32x16_bf16 v[204:219], v[56:59], v[156:159], v[204:219]\n"
    "v_mfma_f32_32x32x16_bf16 v[220:235], v[60:63], v[156:159], v[220:235]\n"
    "s_waitcnt vmcnt(2)\n"
    "v_mfma_f32_32x32x16_bf16 v[204:219], v[64:67], v[160:163], v[204:219]\n"
    "v_mfma_f32_32x32x16_bf16 v[220:235], v[68:71], v[160:163], v[220:235]\n"
    "s_waitcnt vmcnt(1) lgkmcnt(6)\n"
    "v_mfma_f32_32x32x16_bf16 v[204:219], v[72:75], v[164:167], v[204:219]\n"
    "s_waitcnt lgkmcnt(4)\n"
    "v_mfma_f32_32x32x16_bf16 v[220:235], v[76:79], v[164:167], v[220:235]\n"
    "s_waitcnt vmcnt(0) lgkmcnt(2)\n"
    "v_mfma_f32_32x32x16_bf16 v[204:219], v[80:83], v[168:171], v[204:219]\n"
    "s_waitcnt lgkmcnt(0)\n"
    "v_mfma_f32_32x32x16_bf16 v[220:235], v[84:87], v[168:171], v[220:235]\n"
    "s_nop 9\n"
    "v_cvt_pk_bf16_f32 v180, v204, v205\n"
    "v_cvt_pk_bf16_f32 v181, v206, v207\n"
    "ds_write_b64 v254, v[180:181]\n"
    "v_xor_b32 v173, 0x10, v254\n"
    "v_cvt_pk_bf16_f32 v182, v208, v209\n"
    "v_cvt_pk_bf16_f32 v183, v210, v211\n"
    "ds_write_b64 v173, v[182:183]\n"
    "v_xor_b32 v174, 0x20, v254\n"
    "v_cvt_pk_bf16_f32 v184, v212, v213\n"
    "v_cvt_pk_bf16_f32 v185, v214, v215\n"
    "ds_write_b64 v174, v[184:185]\n"
    "v_xor_b32 v175, 0x30, v254\n"
    "v_cvt_pk_bf16_f32 v186, v216, v217\n"
    "v_cvt_pk_bf16_f32 v187, v218, v219\n"
    "ds_write_b64 v175, v[186:187]\n"
    "v_xor_b32 v176, 0x40, v254\n"
    "v_cvt_pk_bf16_f32 v180, v220, v221\n"
    "v_cvt_pk_bf16_f32 v181, v222, v223\n"
    "ds_write_b64 v176, v[180:181]\n"
    "v_xor_b32 v177, 0x50, v254\n"
    "v_cvt_pk_bf16_f32 v182, v224, v225\n"
    "v_cvt_pk_bf16_f32 v183, v226, v227\n"
    "ds_write_b64 v177, v[182:183]\n"
    "v_xor_b32 v178, 0x60, v254\n"
    "v_cvt_pk_bf16_f32 v184, v228, v229\n"
    "v_cvt_pk_bf16_f32 v185, v230, v231\n"
    "ds_write_b64 v178, v[184:185]\n"
    "v_xor_b32 v179, 0x70, v254\n"
    "v_cvt_pk_bf16_f32 v186, v232, v233\n"
    "v_cvt_pk_bf16_f32 v187, v234, v235\n"
    "ds_write_b64 v179, v[186:187]\n"
    "v_mfma_f32_32x32x16_bf16 v[204:219], v[120:123], v[188:191], 0\n"
    "v_mfma_f32_32x32x16_bf16 v[220:235], v[124:127], v[188:191], 0\n"
    "v_mfma_f32_32x32x16_bf16 v[204:219], v[128:131], v[192:195], v[204:219]\n"
    "v_mfma_f32_32x32x16_bf16 v[220:235], v[132:135], v[192:195], v[220:235]\n"
    "s_cmp_lg_u32 s40, 4\n"
    "s_cbranch_scc1 .Lfb_lw33\n"
    "s_cmp_lg_u32 s40, 4\n"
    "s_cbranch_scc1 .Lfb_np35\n"
    "s_add_u32 s56, s54, 0x5100\n"
    "s_mov_b32 s64, 0x30d40\n"
    ".Lfb_pl36:\n"
    "buffer_load_dwordx2 v[2:3], v250, s[12:15], s56 offen sc1\n"
    "s_waitcnt vmcnt(0)\n"
    "v_cmp_ne_u64 vcc, v[2:3], s[46:47]\n"
    "s_cbranch_vccz .Lfb_np35\n"
    "s_sleep 1\n"
    "s_sub_u32 s64, s64, 1\n"
    "s_cbranch_scc0 .Lfb_pl36\n"
    ".Lfb_np35:\n"
    "v_mov_b32 v2, 1\n"
    "ds_write_b32 v255, v2 offset:8\n"
    "s_branch .Lfb_ld34\n"
    ".Lfb_lw33:\n"
    "s_waitcnt lgkmcnt(0)\n"
    ".Lfb_lp37:\n"
    "ds_read_b32 v2, v255 offset:8\n"
    "s_waitcnt lgkmcnt(0)\n"
    "v_readfirstlane_b32 s56, v2\n"
    "s_cmp_eq_u32 s56, 0\n"
    "s_cbranch_scc1 .Lfb_lp37\n"
    ".Lfb_ld34:\n"
    "s_waitcnt lgkmcnt(0)\n"
    "s_cmp_gt_u32 s40, 3\n"
    "s_cbranch_scc0 .Lfb_gr38\n"
    "s_add_u32 s56, s73, s67\n"
    "s_add_u32 s56, s56, 0x1c000\n"
    "buffer_load_dwordx4 v[88:91], v1, s[12:15], s56 offen sc1\n"
    "buffer_load_dwordx4 v[92:95], v1, s[12:15], s56 offen offset:1024 sc1\n"
    "s_add_u32 s56, s73, s68\n"
    "s_add_u32 s56, s56, 0x1c000\n"
    "buffer_load_dwordx4 v[96:99], v1, s[12:15], s56 offen sc1\n"
    "buffer_load_dwordx4 v[100:103], v1, s[12:15], s56 offen offset:1024 sc1\n"
    "s_add_u32 s56, s73, s69\n"
    "s_add_u32 s56, s56, 0x1c000\n"
    "buffer_load_dwordx4 v[104:107], v1, s[12:15], s56 offen sc1\n"
    "buffer_load_dwordx4 v[108:111], v1, s[12:15], s56 offen offset:1024 sc1\n"
    "s_add_u32 s56, s73, s70\n"
    "s_add_u32 s56, s56, 0x1c000\n"
    "buffer_load_dwordx4 v[112:115], v1, s[12:15], s56 offen sc1\n"
    "buffer_load_dwordx4 v[116:119], v1, s[12:15], s56 offen offset:1024 sc1\n"
    "s_add_u32 s56, s73, s71\n"
    "s_add_u32 s56, s56, 0x1c000\n"
    "buffer_load_dwordx4 v[156:159], v1, s[12:15], s56 offen sc1\n"
    "buffer_load_dwordx4 v[160:163], v1, s[12:15], s56 offen offset:1024 sc1\n"
    "s_add_u32 s56, s73, s72\n"
    "s_add_u32 s56, s56, 0x1c000\n"
    "buffer_load_dwordx4 v[164:167], v1, s[12:15], s56 offen sc1\n"
    "buffer_load_dwordx4 v[168:171], v1, s[12:15], s56 offen offset:1024 sc1\n"
    ".Lfb_gr38:\n"
    "s_barrier\n"
    "s_cmp_gt_u32 s40, 3\n"
    "s_cbranch_scc1 .Lfb_gr39\n"
    "s_add_u32 s56, s73, s67\n"
    "s_add_u32 s56, s56, 0x1c000\n"
    "buffer_load_dwordx4 v[88:91], v1, s[12:15], s56 offen sc1\n"
    "buffer_load_dwordx4 v[92:95], v1, s[12:15], s56 offen offset:1024 sc1\n"
    "s_add_u32 s56, s73, s68\n"
    "s_add_u32 s56, s56, 0x1c000\n"
    "buffer_load_dwordx4 v[96:99], v1, s[12:15], s56 offen sc1\n"
    "buffer_load_dwordx4 v[100:103], v1, s[12:15], s56 offen offset:1024 sc1\n"
    "s_add_u32 s56, s73, s69\n"
    "s_add_u32 s56, s56, 0x1c000\n"
    "buffer_load_dwordx4 v[104:107], v1, s[12:15], s56 offen sc1\n"
    "buffer_load_dwordx4 v[108:111], v1, s[12:15], s56 offen offset:1024 sc1\n"
    "s_add_u32 s56, s73, s70\n"
    "s_add_u32 s56, s56, 0x1c000\n"
    "buffer_load_dwordx4 v[112:115], v1, s[12:15], s56 offen sc1\n"
    "buffer_load_dwordx4 v[116:119], v1, s[12:15], s56 offen offset:1024 sc1\n"
    "s_add_u32 s56, s73, s71\n"
    "s_add_u32 s56, s56, 0x1c000\n"
    "buffer_load_dwordx4 v[156:159], v1, s[12:15], s56 offen sc1\n"
    "buffer_load_dwordx4 v[160:163], v1, s[12:15], s56 offen offset:1024 sc1\n"
    "s_add_u32 s56, s73, s72\n"
    "s_add_u32 s56, s56, 0x1c000\n"
    "buffer_load_dwordx4 v[164:167], v1, s[12:15], s56 offen sc1\n"
    "buffer_load_dwordx4 v[168:171], v1, s[12:15], s56 offen offset:1024 sc1\n"
    ".Lfb_gr39:\n"
    "ds_read_b64 v[180:181], v252\n"
    "ds_read_b64 v[182:183], v252 offset:4096\n"
    "ds_read_b64 v[184:185], v252 offset:8192\n"
    "ds_read_b64 v[186:187], v252 offset:12288\n"
    "ds_read_b64 v[152:153], v252 offset:16384\n"
    "ds_read_b64 v[154:155], v252 offset:20480\n"
    "ds_read_b64 v[240:241], v252 offset:24576\n"
    "ds_read_b64 v[242:243], v252 offset:28672\n"
    "ds_read_b64 v[236:237], v245\n"
    "v_mov_b32 v2, 0\n"
    "v_mov_b32 v3, 0\n"
    "v_mov_b32 v4, 0\n"
    "v_mov_b32 v5, 0\n"
    "s_waitcnt lgkmcnt(8)\n"
    "v_dot2c_f32_bf16 v2, v203, v180\n"
    "v_dot2c_f32_bf16 v3, v249, v180\n"
    "v_dot2c_f32_bf16 v4, v203, v181\n"
    "v_dot2c_f32_bf16 v5, v249, v181\n"
    "s_waitcnt lgkmcnt(7)\n"
    "v_dot2c_f32_bf16 v2, v203, v182\n"
    "v_dot2c_f32_bf16 v3, v249, v182\n"
    "v_dot2c_f32_bf16 v4, v203, v183\n"
    "v_dot2c_f32_bf16 v5, v249, v183\n"
    "s_waitcnt lgkmcnt(6)\n"
    "v_dot2c_f32_bf16 v2, v203, v184\n"
    "v_dot2c_f32_bf16 v3, v249, v184\n"
    "v_dot2c_f32_bf16 v4, v203, v185\n"
    "v_dot2c_f32_bf16 v5, v249, v185\n"
    "s_waitcnt lgkmcnt(5)\n"
    "v_dot2c_f32_bf16 v2, v203, v186\n"
    "v_dot2c_f32_bf16 v3, v249, v186\n"
    "v_dot2c_f32_bf16 v4, v203, v187\n"
    "v_dot2c_f32_bf16 v5, v249, v187\n"
    "s_waitcnt lgkmcnt(4)\n"
    "v_dot2c_f32_bf16 v2, v203, v152\n"
    "v_dot2c_f32_bf16 v3, v249, v152\n"
    "v_dot2c_f32_bf16 v4, v203, v153\n"
    "v_dot2c_f32_bf16 v5, v249, v153\n"
    "s_waitcnt lgkmcnt(3)\n"
    "v_dot2c_f32_bf16 v2, v203, v154\n"
    "v_dot2c_f32_bf16 v3, v249, v154\n"
    "v_dot2c_f32_bf16 v4, v203, v155\n"
    "v_dot2c_f32_bf16 v5, v249, v155\n"
    "s_waitcnt lgkmcnt(2)\n"
    "v_dot2c_f32_bf16 v2, v203, v240\n"
    "v_dot2c_f32_bf16 v3, v249, v240\n"
    "v_dot2c_f32_bf16 v4, v203, v241\n"
    "v_dot2c_f32_bf16 v5, v249, v241\n"
    "s_waitcnt lgkmcnt(1)\n"
    "v_dot2c_f32_bf16 v2, v203, v242\n"
    "v_dot2c_f32_bf16 v3, v249, v242\n"
    "v_dot2c_f32_bf16 v4, v203, v243\n"
    "v_dot2c_f32_bf16 v5, v249, v243\n"
    "s_waitcnt lgkmcnt(0)\n"
    "v_dot2c_f32_bf16 v2, v203, v236\n"
    "v_dot2c_f32_bf16 v3, v249, v236\n"
    "v_dot2c_f32_bf16 v4, v203, v237\n"
    "v_dot2c_f32_bf16 v5, v249, v237\n"
    "s_nop 0\n"
    "v_cvt_pk_bf16_f32 v2, v2, v3\n"
    "s_nop 0\n"
    "v_cvt_pk_bf16_f32 v3, v4, v5\n"
    "buffer_store_dwordx2 v[2:3], v246, s[36:39], s77 offen sc1\n"
    "s_waitcnt vmcnt(12)\n"
    "v_mfma_f32_32x32x16_bf16 v[204:219], v[136:139], v[88:91], v[204:219]\n"
    "v_mfma_f32_32x32x16_bf16 v[220:235], v[140:143], v[88:91], v[220:235]\n"
    "s_waitcnt vmcnt(11)\n"
    "v_mfma_f32_32x32x16_bf16 v[204:219], v[144:147], v[92:95], v[204:219]\n"
    "v_mfma_f32_32x32x16_bf16 v[220:235], v[148:151], v[92:95], v[220:235]\n"
    "s_waitcnt vmcnt(10)\n"
    "v_mfma_f32_32x32x16_bf16 v[204:219], v[8:11], v[96:99], v[204:219]\n"
    "v_mfma_f32_32x32x16_bf16 v[220:235], v[12:15], v[96:99], v[220:235]\n"
    "s_waitcnt vmcnt(9)\n"
    "v_mfma_f32_32x32x16_bf16 v[204:219], v[16:19], v[100:103], v[204:219]\n"
    "v_mfma_f32_32x32x16_bf16 v[220:235], v[20:23], v[100:103], v[220:235]\n"
    "s_waitcnt vmcnt(8)\n"
    "v_mfma_f32_32x32x16_bf16 v[204:219], v[24:27], v[104:107], v[204:219]\n"
    "v_mfma_f32_32x32x16_bf16 v[220:235], v[28:31], v[104:107], v[220:235]\n"
    "s_waitcnt vmcnt(7)\n"
    "v_mfma_f32_32x32x16_bf16 v[204:219], v[32:35], v[108:111], v[204:219]\n"
    "v_mfma_f32_32x32x16_bf16 v[220:235], v[36:39], v[108:111], v[220:235]\n"
    "s_waitcnt vmcnt(6)\n"
    "v_mfma_f32_32x32x16_bf16 v[204:219], v[40:43], v[112:115], v[204:219]\n"
    "v_mfma_f32_32x32x16_bf16 v[220:235], v[44:47], v[112:115], v[220:235]\n"
    "s_waitcnt vmcnt(5)\n"
    "v_mfma_f32_32x32x16_bf16 v[204:219], v[48:51], v[116:119], v[204:219]\n"
    "v_mfma_f32_32x32x16_bf16 v[220:235], v[52:55], v[116:119], v[220:235]\n"
    "s_waitcnt vmcnt(4)\n"
    "v_mfma_f32_32x32x16_bf16 v[204:219], v[56:59], v[156:159], v[204:219]\n"
    "v_mfma_f32_32x32x16_bf16 v[220:235], v[60:63], v[156:159], v[220:235]\n"
    "s_waitcnt vmcnt(3)\n"
    "v_mfma_f32_32x32x16_bf16 v[204:219], v[64:67], v[160:163], v[204:219]\n"
    "v_mfma_f32_32x32x16_bf16 v[220:235], v[68:71], v[160:163], v[220:235]\n"
    "s_waitcnt vmcnt(2)\n"
    "v_mfma_f32_32x32x16_bf16 v[204:219], v[72:75], v[164:167], v[204:219]\n"
    "v_mfma_f32_32x32x16_bf16 v[220:235], v[76:79], v[164:167], v[220:235]\n"
    "s_waitcnt vmcnt(1)\n"
    "v_mfma_f32_32x32x16_bf16 v[204:219], v[80:83], v[168:171], v[204:219]\n"
    "v_mfma_f32_32x32x16_bf16 v[220:235], v[84:87], v[168:171], v[220:235]\n"
    "s_nop 10\n"
    "v_cvt_pk_bf16_f32 v188, v204, v205\n"
    "v_cvt_pk_bf16_f32 v189, v206, v207\n"
    "ds_write_b64 v254, v[188:189] offset:32768\n"
    "v_xor_b32 v173, 0x10, v254\n"
    "v_cvt_pk_bf16_f32 v190, v208, v209\n"
    "v_cvt_pk_bf16_f32 v191, v210, v211\n"
    "ds_write_b64 v173, v[190:191] offset:32768\n"
    "v_xor_b32 v174, 0x20, v254\n"
    "v_cvt_pk_bf16_f32 v192, v212, v213\n"
    "v_cvt_pk_bf16_f32 v193, v214, v215\n"
    "ds_write_b64 v174, v[192:193] offset:32768\n"
    "v_xor_b32 v175, 0x30, v254\n"
    "v_cvt_pk_bf16_f32 v194, v216, v217\n"
    "v_cvt_pk_bf16_f32 v195, v218, v219\n"
    "ds_write_b64 v175, v[194:195] offset:32768\n"
    "v_xor_b32 v176, 0x40, v254\n"
    "v_cvt_pk_bf16_f32 v188, v220, v221\n"
    "v_cvt_pk_bf16_f32 v189, v222, v223\n"
    "ds_write_b64 v176, v[188:189] offset:32768\n"
    "v_xor_b32 v177, 0x50, v254\n"
    "v_cvt_pk_bf16_f32 v190, v224, v225\n"
    "v_cvt_pk_bf16_f32 v191, v226, v227\n"
    "ds_write_b64 v177, v[190:191] offset:32768\n"
    "v_xor_b32 v178, 0x60, v254\n"
    "v_cvt_pk_bf16_f32 v192, v228, v229\n"
    "v_cvt_pk_bf16_f32 v193, v230, v231\n"
    "ds_write_b64 v178, v[192:193] offset:32768\n"
    "v_xor_b32 v179, 0x70, v254\n"
    "v_cvt_pk_bf16_f32 v194, v232, v233\n"
    "v_cvt_pk_bf16_f32 v195, v234, v235\n"
    "ds_write_b64 v179, v[194:195] offset:32768\n"
    "s_waitcnt lgkmcnt(0)\n"
    "s_barrier\n"
    "ds_read_b64 v[180:181], v252 offset:32768\n"
    "ds_read_b64 v[182:183], v252 offset:36864\n"
    "ds_read_b64 v[184:185], v252 offset:40960\n"
    "ds_read_b64 v[186:187], v252 offset:45056\n"
    "ds_read_b64 v[152:153], v252 offset:49152\n"
    "ds_read_b64 v[154:155], v252 offset:53248\n"
    "ds_read_b64 v[240:241], v252 offset:57344\n"
    "ds_read_b64 v[242:243], v252 offset:61440\n"
    "ds_read_b64 v[236:237], v245 offset:49152\n"
    "v_mov_b32 v2, 0\n"
    "v_mov_b32 v3, 0\n"
    "v_mov_b32 v4, 0\n"
    "v_mov_b32 v5, 0\n"
    "s_waitcnt lgkmcnt(8)\n"
    "v_dot2c_f32_bf16 v2, v203, v180\n"
    "v_dot2c_f32_bf16 v3, v249, v180\n"
    "v_dot2c_f32_bf16 v4, v203, v181\n"
    "v_dot2c_f32_bf16 v5, v249, v181\n"
    "s_waitcnt lgkmcnt(7)\n"
    "v_dot2c_f32_bf16 v2, v203, v182\n"
    "v_dot2c_f32_bf16 v3, v249, v182\n"
    "v_dot2c_f32_bf16 v4, v203, v183\n"
    "v_dot2c_f32_bf16 v5, v249, v183\n"
    "s_waitcnt lgkmcnt(6)\n"
    "v_dot2c_f32_bf16 v2, v203, v184\n"
    "v_dot2c_f32_bf16 v3, v249, v184\n"
    "v_dot2c_f32_bf16 v4, v203, v185\n"
    "v_dot2c_f32_bf16 v5, v249, v185\n"
    "s_waitcnt lgkmcnt(5)\n"
    "v_dot2c_f32_bf16 v2, v203, v186\n"
    "v_dot2c_f32_bf16 v3, v249, v186\n"
    "v_dot2c_f32_bf16 v4, v203, v187\n"
    "v_dot2c_f32_bf16 v5, v249, v187\n"
    "s_waitcnt lgkmcnt(4)\n"
    "v_dot2c_f32_bf16 v2, v203, v152\n"
    "v_dot2c_f32_bf16 v3, v249, v152\n"
    "v_dot2c_f32_bf16 v4, v203, v153\n"
    "v_dot2c_f32_bf16 v5, v249, v153\n"
    "s_waitcnt lgkmcnt(3)\n"
    "v_dot2c_f32_bf16 v2, v203, v154\n"
    "v_dot2c_f32_bf16 v3, v249, v154\n"
    "v_dot2c_f32_bf16 v4, v203, v155\n"
    "v_dot2c_f32_bf16 v5, v249, v155\n"
    "s_waitcnt lgkmcnt(2)\n"
    "v_dot2c_f32_bf16 v2, v203, v240\n"
    "v_dot2c_f32_bf16 v3, v249, v240\n"
    "v_dot2c_f32_bf16 v4, v203, v241\n"
    "v_dot2c_f32_bf16 v5, v249, v241\n"
    "s_waitcnt lgkmcnt(1)\n"
    "v_dot2c_f32_bf16 v2, v203, v242\n"
    "v_dot2c_f32_bf16 v3, v249, v242\n"
    "v_dot2c_f32_bf16 v4, v203, v243\n"
    "v_dot2c_f32_bf16 v5, v249, v243\n"
    "s_waitcnt lgkmcnt(0)\n"
    "v_dot2c_f32_bf16 v2, v203, v236\n"
    "v_dot2c_f32_bf16 v3, v249, v236\n"
    "v_dot2c_f32_bf16 v4, v203, v237\n"
    "v_dot2c_f32_bf16 v5, v249, v237\n"
    "s_nop 0\n"
    "v_cvt_pk_bf16_f32 v2, v2, v3\n"
    "s_nop 0\n"
    "v_cvt_pk_bf16_f32 v3, v4, v5\n"
    "s_add_u32 s77, s77, 0x7000\n"
    "buffer_store_dwordx2 v[2:3], v246, s[36:39], s77 offen sc1\n"
    "s_cmp_lg_u32 s65, 0\n"
    "s_cbranch_scc1 .Lfb_ne40\n"
    "s_mov_b32 s58, 0\n"
    "s_cmp_lg_u32 s40, 0\n"
    "s_cbranch_scc1 .Lfb_nf41\n"
    "s_mov_b32 s56, s58\n"
    "s_mov_b64 exec, 1\n"
    "buffer_store_dwordx2 v[6:7], off, s[12:15], s56 sc1\n"
    "s_mov_b64 exec, -1\n"
    ".Lfb_nf41:\n"
    ".Lfb_ne40:\n"
    ".Lfb_done:\n"
    "s_endpgm\n"
    ".Lfb_idle:\n"
    "v_min_u32 v2, 27, v202\n"
    "v_mul_u32_u24 v3, 0xe00, v201\n"
    "v_lshl_add_u32 v2, v2, 2, v3\n"
    "s_mul_i32 s56, s40, 200704\n"
    "s_mul_i32 s57, s43, 112\n"
    "s_add_u32 s56, s56, s57\n"
    "buffer_load_dword v8, v2, s[20:23], s56 offen\n"
    "s_add_u32 s56, s56, 0x1c00\n"
    "buffer_load_dword v9, v2, s[20:23], s56 offen\n"
    "s_add_u32 s56, s56, 0x1c00\n"
    "buffer_load_dword v10, v2, s[20:23], s56 offen\n"
    "s_add_u32 s56, s56, 0x1c00\n"
    "buffer_load_dword v11, v2, s[20:23], s56 offen\n"
    "s_add_u32 s56, s56, 0x1c00\n"
    "buffer_load_dword v12, v2, s[20:23], s56 offen\n"
    "s_add_u32 s56, s56, 0x1c00\n"
    "buffer_load_dword v13, v2, s[20:23], s56 offen\n"
    "s_add_u32 s56, s56, 0x1c00\n"
    "buffer_load_dword v14, v2, s[20:23], s56 offen\n"
    "s_add_u32 s56, s56, 0x1c00\n"
    "buffer_load_dword v15, v2, s[20:23], s56 offen\n"
    "s_add_u32 s56, s56, 0x1c00\n"
    "buffer_load_dword v16, v2, s[20:23], s56 offen\n"
    "s_add_u32 s56, s56, 0x1c00\n"
    "buffer_load_dword v17, v2, s[20:23], s56 offen\n"
    "s_add_u32 s56, s56, 0x1c00\n"
    "buffer_load_dword v18, v2, s[20:23], s56 offen\n"
    "s_add_u32 s56, s56, 0x1c00\n"
    "buffer_load_dword v19, v2, s[20:23], s56 offen\n"
    "s_add_u32 s56, s56, 0x1c00\n"
    "buffer_load_dword v20, v2, s[20:23], s56 offen\n"
    "s_add_u32 s56, s56, 0x1c00\n"
    "buffer_load_dword v21, v2, s[20:23], s56 offen\n"
    "s_add_u32 s56, s56, 0x1c00\n"
    "buffer_load_dword v22, v2, s[20:23], s56 offen\n"
    "s_add_u32 s56, s56, 0x1c00\n"
    "buffer_load_dword v23, v2, s[20:23], s56 offen\n"
    "s_add_u32 s56, s56, 0x1c00\n"
    "buffer_load_dword v24, v2, s[20:23], s56 offen\n"
    "s_add_u32 s56, s56, 0x1c00\n"
    "buffer_load_dword v25, v2, s[20:23], s56 offen\n"
    "s_add_u32 s56, s56, 0x1c00\n"
    "buffer_load_dword v26, v2, s[20:23], s56 offen\n"
    "s_add_u32 s56, s56, 0x1c00\n"
    "buffer_load_dword v27, v2, s[20:23], s56 offen\n"
    "s_add_u32 s56, s56, 0x1c00\n"
    "buffer_load_dword v28, v2, s[20:23], s56 offen\n"
    "s_add_u32 s56, s56, 0x1c00\n"
    "buffer_load_dword v29, v2, s[20:23], s56 offen\n"
    "s_add_u32 s56, s56, 0x1c00\n"
    "buffer_load_dword v30, v2, s[20:23], s56 offen\n"
    "s_add_u32 s56, s56, 0x1c00\n"
    "buffer_load_dword v31, v2, s[20:23], s56 offen\n"
    "s_add_u32 s56, s56, 0x1c00\n"
    "buffer_load_dword v32, v2, s[20:23], s56 offen\n"
    "s_add_u32 s56, s56, 0x1c00\n"
    "buffer_load_dword v33, v2, s[20:23], s56 offen\n"
    "s_add_u32 s56, s56, 0x1c00\n"
    "buffer_load_dword v34, v2, s[20:23], s56 offen\n"
    "s_add_u32 s56, s56, 0x1c00\n"
    "buffer_load_dword v35, v2, s[20:23], s56 offen\n"
    "s_add_u32 s56, s56, 0x1c00\n"
    "v_mov_b32 v40, 0x3f80\n"
    "v_mov_b32 v41, 0x3f800000\n"
    "v_mov_b32 v42, 0\n"
    "v_mov_b32 v43, 0\n"
    "s_waitcnt vmcnt(27)\n"
    "v_dot2c_f32_bf16 v42, v40, v8\n"
    "v_dot2c_f32_bf16 v43, v41, v8\n"
    "s_waitcnt vmcnt(26)\n"
    "v_dot2c_f32_bf16 v42, v40, v9\n"
    "v_dot2c_f32_bf16 v43, v41, v9\n"
    "s_waitcnt vmcnt(25)\n"
    "v_dot2c_f32_bf16 v42, v40, v10\n"
    "v_dot2c_f32_bf16 v43, v41, v10\n"
    "s_waitcnt vmcnt(24)\n"
    "v_dot2c_f32_bf16 v42, v40, v11\n"
    "v_dot2c_f32_bf16 v43, v41, v11\n"
    "s_waitcnt vmcnt(23)\n"
    "v_dot2c_f32_bf16 v42, v40, v12\n"
    "v_dot2c_f32_bf16 v43, v41, v12\n"
    "s_waitcnt vmcnt(22)\n"
    "v_dot2c_f32_bf16 v42, v40, v13\n"
    "v_dot2c_f32_bf16 v43, v41, v13\n"
    "s_waitcnt vmcnt(21)\n"
    "v_dot2c_f32_bf16 v42, v40, v14\n"
    "v_dot2c_f32_bf16 v43, v41, v14\n"
    "s_waitcnt vmcnt(20)\n"
    "v_dot2c_f32_bf16 v42, v40, v15\n"
    "v_dot2c_f32_bf16 v43, v41, v15\n"
    "s_waitcnt vmcnt(19)\n"
    "v_dot2c_f32_bf16 v42, v40, v16\n"
    "v_dot2c_f32_bf16 v43, v41, v16\n"
    "s_waitcnt vmcnt(18)\n"
    "v_dot2c_f32_bf16 v42, v40, v17\n"
    "v_dot2c_f32_bf16 v43, v41, v17\n"
    "s_waitcnt vmcnt(17)\n"
    "v_dot2c_f32_bf16 v42, v40, v18\n"
    "v_dot2c_f32_bf16 v43, v41, v18\n"
    "s_waitcnt vmcnt(16)\n"
    "v_dot2c_f32_bf16 v42, v40, v19\n"
    "v_dot2c_f32_bf16 v43, v41, v19\n"
    "s_waitcnt vmcnt(15)\n"
    "v_dot2c_f32_bf16 v42, v40, v20\n"
    "v_dot2c_f32_bf16 v43, v41, v20\n"
    "s_waitcnt vmcnt(14)\n"
    "v_dot2c_f32_bf16 v42, v40, v21\n"
    "v_dot2c_f32_bf16 v43, v41, v21\n"
    "s_waitcnt vmcnt(13)\n"
    "v_dot2c_f32_bf16 v42, v40, v22\n"
    "v_dot2c_f32_bf16 v43, v41, v22\n"
    "s_waitcnt vmcnt(12)\n"
    "v_dot2c_f32_bf16 v42, v40, v23\n"
    "v_dot2c_f32_bf16 v43, v41, v23\n"
    "s_waitcnt vmcnt(11)\n"
    "v_dot2c_f32_bf16 v42, v40, v24\n"
    "v_dot2c_f32_bf16 v43, v41, v24\n"
    "s_waitcnt vmcnt(10)\n"
    "v_dot2c_f32_bf16 v42, v40, v25\n"
    "v_dot2c_f32_bf16 v43, v41, v25\n"
    "s_waitcnt vmcnt(9)\n"
    "v_dot2c_f32_bf16 v42, v40, v26\n"
    "v_dot2c_f32_bf16 v43, v41, v26\n"
    "s_waitcnt vmcnt(8)\n"
    "v_dot2c_f32_bf16 v42, v40, v27\n"
    "v_dot2c_f32_bf16 v43, v41, v27\n"
    "s_waitcnt vmcnt(7)\n"
    "v_dot2c_f32_bf16 v42, v40, v28\n"
    "v_dot2c_f32_bf16 v43, v41, v28\n"
    "s_waitcnt vmcnt(6)\n"
    "v_dot2c_f32_bf16 v42, v40, v29\n"
    "v_dot2c_f32_bf16 v43, v41, v29\n"
    "s_waitcnt vmcnt(5)\n"
    "v_dot2c_f32_bf16 v42, v40, v30\n"
    "v_dot2c_f32_bf16 v43, v41, v30\n"
    "s_waitcnt vmcnt(4)\n"
    "v_dot2c_f32_bf16 v42, v40, v31\n"
    "v_dot2c_f32_bf16 v43, v41, v31\n"
    "s_waitcnt vmcnt(3)\n"
    "v_dot2c_f32_bf16 v42, v40, v32\n"
    "v_dot2c_f32_bf16 v43, v41, v32\n"
    "s_waitcnt vmcnt(2)\n"
    "v_dot2c_f32_bf16 v42, v40, v33\n"
    "v_dot2c_f32_bf16 v43, v41, v33\n"
    "s_waitcnt vmcnt(1)\n"
    "v_dot2c_f32_bf16 v42, v40, v34\n"
    "v_dot2c_f32_bf16 v43, v41, v34\n"
    "s_waitcnt vmcnt(0)\n"
    "v_dot2c_f32_bf16 v42, v40, v35\n"
    "v_dot2c_f32_bf16 v43, v41, v35\n"
    "s_lshl_b32 s56, s40, 9\n"
    "v_lshlrev_b32 v3, 3, v196\n"
    "v_add_u32 v2, s56, v3\n"
    "ds_write_b64 v2, v[42:43]\n"
    "s_waitcnt lgkmcnt(0)\n"
    "s_barrier\n"
    "s_cmp_lg_u32 s40, 0\n"
    "s_cbranch_scc1 .Lfb_idle2\n"
    "v_lshlrev_b32 v3, 3, v202\n"
    "ds_read_b64 v[8:9], v3\n"
    "ds_read_b64 v[10:11], v3 offset:256\n"
    "ds_read_b64 v[12:13], v3 offset:512\n"
    "ds_read_b64 v[14:15], v3 offset:768\n"
    "ds_read_b64 v[16:17], v3 offset:1024\n"
    "ds_read_b64 v[18:19], v3 offset:1280\n"
    "ds_read_b64 v[20:21], v3 offset:1536\n"
    "ds_read_b64 v[22:23], v3 offset:1792\n"
    "ds_read_b64 v[24:25], v3 offset:2048\n"
    "ds_read_b64 v[26:27], v3 offset:2304\n"
    "ds_read_b64 v[28:29], v3 offset:2560\n"
    "ds_read_b64 v[30:31], v3 offset:2816\n"
    "ds_read_b64 v[32:33], v3 offset:3072\n"
    "ds_read_b64 v[34:35], v3 offset:3328\n"
    "ds_read_b64 v[36:37], v3 offset:3584\n"
    "ds_read_b64 v[38:39], v3 offset:3840\n"
    "s_waitcnt lgkmcnt(14)\n"
    "v_pk_add_f32 v[8:9], v[8:9], v[10:11]\n"
    "s_waitcnt lgkmcnt(13)\n"
    "v_pk_add_f32 v[8:9], v[8:9], v[12:13]\n"
    "s_waitcnt lgkmcnt(12)\n"
    "v_pk_add_f32 v[8:9], v[8:9], v[14:15]\n"
    "s_waitcnt lgkmcnt(11)\n"
    "v_pk_add_f32 v[8:9], v[8:9], v[16:17]\n"
    "s_waitcnt lgkmcnt(10)\n"
    "v_pk_add_f32 v[8:9], v[8:9], v[18:19]\n"
    "s_waitcnt lgkmcnt(9)\n"
    "v_pk_add_f32 v[8:9], v[8:9], v[20:21]\n"
    "s_waitcnt lgkmcnt(8)\n"
    "v_pk_add_f32 v[8:9], v[8:9], v[22:23]\n"
    "s_waitcnt lgkmcnt(7)\n"
    "v_pk_add_f32 v[8:9], v[8:9], v[24:25]\n"
    "s_waitcnt lgkmcnt(6)\n"
    "v_pk_add_f32 v[8:9], v[8:9], v[26:27]\n"
    "s_waitcnt lgkmcnt(5)\n"
    "v_pk_add_f32 v[8:9], v[8:9], v[28:29]\n"
    "s_waitcnt lgkmcnt(4)\n"
    "v_pk_add_f32 v[8:9], v[8:9], v[30:31]\n"
    "s_waitcnt lgkmcnt(3)\n"
    "v_pk_add_f32 v[8:9], v[8:9], v[32:33]\n"
    "s_waitcnt lgkmcnt(2)\n"
    "v_pk_add_f32 v[8:9], v[8:9], v[34:35]\n"
    "s_waitcnt lgkmcnt(1)\n"
    "v_pk_add_f32 v[8:9], v[8:9], v[36:37]\n"
    "s_waitcnt lgkmcnt(0)\n"
    "v_pk_add_f32 v[8:9], v[8:9], v[38:39]\n"
    "s_mul_i32 s56, s43, 224\n"
    "s_add_u32 s56, s56, 0xa000\n"
    "s_mov_b64 exec, 0xfffffff\n"
    "buffer_store_dwordx2 v[8:9], v3, s[12:15], s56 offen sc1\n"
    "s_waitcnt vmcnt(0) lgkmcnt(0)\n"
    "s_lshl_b32 s58, s43, 3\n"
    "s_cmp_lg_u32 s40, 0\n"
    "s_cbranch_scc1 .Lfb_nf42\n"
    "s_add_u32 s56, s58, 0x9000\n"
    "s_mov_b64 exec, 1\n"
    "buffer_store_dwordx2 v[6:7], off, s[12:15], s56 sc1\n"
    "s_mov_b64 exec, -1\n"
    ".Lfb_nf42:\n"
    ".Lfb_idle2:\n"
    "s_endpgm\n"
    ".Lfb_end:\n"
    :
    : "s"(__builtin_amdgcn_kernarg_segment_ptr()), "s"(blockIdx.x), "v"(threadIdx.x),
      "s"((u32)(size_t)(__attribute__((address_space(3))) char*)g_lds)
    : "v1", "v2", "v3", "v4", "v5", "v6", "v7", "v8", "v9", "v10", "v11", "v12", "v13", "v14", "v15", "v16", "v17", "v18", "v19", "v20", "v21", "v22", "v23", "v24", "v25", "v26", "v27", "v28", "v29", "v30", "v31", "v32", "v33", "v34", "v35", "v36", "v37", "v38", "v39", "v40", "v41", "v42", "v43", "v44", "v45", "v46", "v47", "v48", "v49", "v50", "v51", "v52", "v53", "v54", "v55", "v56", "v57", "v58", "v59", "v60", "v61", "v62", "v63", "v64", "v65", "v66", "v67", "v68", "v69", "v70", "v71", "v72", "v73", "v74", "v75", "v76", "v77", "v78", "v79", "v80", "v81", "v82", "v83", "v84", "v85", "v86", "v87", "v88", "v89", "v90", "v91", "v92", "v93", "v94", "v95", "v96", "v97", "v98", "v99", "v100", "v101", "v102", "v103", "v104", "v105", "v106", "v107", "v108", "v109", "v110", "v111", "v112", "v113", "v114", "v115", "v116", "v117", "v118", "v119", "v120", "v121", "v122", "v123", "v124", "v125", "v126", "v127", "v128", "v129", "v130", "v131", "v132", "v133", "v134", "v135", "v136", "v137", "v138", "v139", "v140", "v141", "v142", "v143", "v144", "v145", "v146", "v147", "v148", "v149", "v150", "v151", "v152", "v153", "v154", "v155", "v156", "v157", "v158", "v159", "v160", "v161", "v162", "v163", "v164", "v165", "v166", "v167", "v168", "v169", "v170", "v171", "v172", "v173", "v174", "v175", "v176", "v177", "v178", "v179", "v180", "v181", "v182", "v183", "v184", "v185", "v186", "v187", "v188", "v189", "v190", "v191", "v192", "v193", "v194", "v195", "v196", "v197", "v198", "v199", "v200", "v201", "v202", "v203", "v204", "v205", "v206", "v207", "v208", "v209", "v210", "v211", "v212", "v213", "v214", "v215", "v216", "v217", "v218", "v219", "v220", "v221", "v222", "v223", "v224", "v225", "v226", "v227", "v228", "v229", "v230", "v231", "v232", "v233", "v234", "v235", "v236", "v237", "v238", "v239", "v240", "v241", "v242", "v243", "v244", "v245", "v246", "v247", "v248", "v249", "v250", "v251", "v252", "v253", "v254", "v255", "s4", "s5", "s6", "s7", "s8", "s9", "s10", "s11", "s12", "s13", "s14", "s15", "s16", "s17", "s18", "s19", "s20", "s21", "s22", "s23", "s24", "s25", "s26", "s27", "s28", "s29", "s30", "s31", "s33", "s34", "s35", "s36", "s37", "s38", "s39", "s40", "s41", "s42", "s43", "s44", "s45", "s46", "s47", "s48", "s49", "s50", "s51", "s52", "s53", "s54", "s55", "s56", "s57", "s58", "s59", "s60", "s61", "s62", "s63", "s64", "s65", "s66", "s67", "s68", "s69", "s70", "s71", "s72", "s73", "s74", "s75", "s76", "s77", "s78", "s79", "vcc", "scc", "memory");
}

extern "C" void kernel_launch(void* const* d_in, const int* in_sizes, int n_in, void* d_out, int out_size,
                              void* d_ws, size_t ws_size, hipStream_t stream) {
  constexpr u32 F = 0x00020000u;
  fused_block<<<256, 512, 0, stream>>>(d_in[0], 2048u * 896u, F, d_in[1], 448u * 2688u, F, d_ws, WS_END, F,
                                       d_in[2], 16u * 1568u, F, d_in[5], 448u * 3584u, F, d_in[7], 1792u * 896u, F,
                                       d_in[3], 448u * 896u, F, d_in[6], 3584u, F, d_out, 2048u * 896u, F);
}
```
